```python
import torch

def _softmax(x, axis=-1):
    return torch.softmax(x, dim=axis)
def _gelu(x):
    return torch.nn.functional.gelu(x, approximate="tanh")
def _silu(x):
    return torch.nn.functional.silu(x)
def _sigmoid(x):
    return torch.sigmoid(x)
def _layernorm(x, axis=-1, eps=1e-5):
    m = x.mean(dim=axis, keepdim=True)
    v = x.var(dim=axis, keepdim=True, unbiased=False)
    return (x - m) / torch.sqrt(v + eps)
def _scatter_add(tgt, idx, src, axis=0):
    return tgt.clone().index_add_(axis, idx, src)
def _segment_sum(src, seg, n):
    out = torch.zeros((n,) + src.shape[1:], dtype=src.dtype)
    return out.index_add_(0, seg, src)
def _pad(x, pw):
    flat = tuple(v for pair in reversed(pw) for v in pair)
    return torch.nn.functional.pad(x, flat)

kernel_name = 'standalone__mha_block_cached__near_ridge'

def setup_inputs():

    _w = (lambda a: (lambda t: t.to(torch.bfloat16) if t.is_floating_point() else t)(torch.as_tensor(a)))
    B, Sq, D = 8, 64, 640
    H, Sk, Dh = 32, 64, 192
    return {
        "x": _w((lambda *s: torch.randn(*s, dtype=torch.bfloat16) * 0.8)(B, Sq, D)),
        "k_cache": _w((lambda *s: torch.randn(*s, dtype=torch.bfloat16) * 0.8)(B, H, Sk, Dh)),
        "v_cache": _w((lambda *s: torch.randn(*s, dtype=torch.bfloat16) * 0.05)(B, H, Sk, Dh)),
        "wq": _w((lambda *s: torch.randn(*s, dtype=torch.bfloat16) * 0.8)(D, H * Dh) * (D ** -0.5)),
        "wo": _w((lambda *s: torch.randn(*s, dtype=torch.bfloat16) * 0.05)(H * Dh, D) * ((H * Dh) ** -0.5)),
    }

def reference(x, k_cache, v_cache, wq, wo):
    B, Sq, D = x.shape
    H, Dh = 32, 192
    q = (lambda x, *d: x.permute(*d))((x @ wq).reshape(B, Sq, H, Dh), 0, 2, 1, 3)
    scores = torch.einsum("bhqd,bhkd->bhqk", q, k_cache) * (Dh ** -0.5)
    p = _softmax(scores, axis=-1)
    o = torch.einsum("bhqk,bhkd->bhqd", p, v_cache)
    return (lambda x, *d: x.permute(*d))(o, 0, 2, 1, 3).reshape(B, Sq, H * Dh) @ wo
```

```cpp
#include <hip/hip_runtime.h>
#include <hip/hip_bf16.h>

typedef unsigned short u16;
__device__ __forceinline__ float bf2f(u16 b) { return __uint_as_float(((unsigned)b) << 16); }
__device__ __forceinline__ u16 f2bf(float f) { __hip_bfloat16 h = __float2bfloat16(f); return *(u16*)&h; }

constexpr int B = 8, SQ = 64, D = 640, H = 32, SK = 64, DH = 192, HD = H * DH;

__global__ void k_q(const u16* x, const u16* wq, u16* q) {
    int n = blockIdx.x * 256 + threadIdx.x;
    int m = blockIdx.y;
    float acc = 0.f;
    for (int k = 0; k < D; ++k) acc += bf2f(x[m * D + k]) * bf2f(wq[(size_t)k * HD + n]);
    q[(size_t)m * HD + n] = f2bf(acc);
}
__global__ void k_attn(const u16* q, const u16* kc, const u16* vc, u16* o) {
    int bh = blockIdx.x / SQ, qr = blockIdx.x % SQ;
    int b = bh / H, h = bh % H;
    int t = threadIdx.x;
    __shared__ float p[SK];
    const u16* qp = q + (size_t)(b * SQ + qr) * HD + h * DH;
    const u16* kp = kc + ((size_t)bh * SK + t) * DH;
    float s = 0.f;
    for (int d = 0; d < DH; ++d) s += bf2f(qp[d]) * bf2f(kp[d]);
    s *= 0.07216878364870322f;
    float m = s;
    for (int off = 32; off; off >>= 1) m = fmaxf(m, __shfl_xor(m, off));
    float e = __expf(s - m);
    float sum = e;
    for (int off = 32; off; off >>= 1) sum += __shfl_xor(sum, off);
    p[t] = bf2f(f2bf(e / sum));
    __syncthreads();
    for (int d = t; d < DH; d += 64) {
        float acc = 0.f;
        for (int k = 0; k < SK; ++k) acc += p[k] * bf2f(vc[((size_t)bh * SK + k) * DH + d]);
        o[(size_t)(b * SQ + qr) * HD + h * DH + d] = f2bf(acc);
    }
}
__global__ void k_o(const u16* o, const u16* wo, u16* out) {
    int n = blockIdx.x * 128 + threadIdx.x;
    int m = blockIdx.y;
    float acc = 0.f;
    for (int k = 0; k < HD; ++k) acc += bf2f(o[(size_t)m * HD + k]) * bf2f(wo[(size_t)k * D + n]);
    out[m * D + n] = f2bf(acc);
}

extern "C" void kernel_launch(void* const* d_in, const int* in_sizes, int n_in,
                              void* d_out, int out_size, void* d_ws, size_t ws_size,
                              hipStream_t stream) {
    const u16* x = (const u16*)d_in[0];
    const u16* kc = (const u16*)d_in[1];
    const u16* vc = (const u16*)d_in[2];
    const u16* wq = (const u16*)d_in[3];
    const u16* wo = (const u16*)d_in[4];
    u16* q = (u16*)d_ws;
    u16* o = q + (size_t)B * SQ * HD;
    k_q<<<dim3(HD / 256, B * SQ), 256, 0, stream>>>(x, wq, q);
    k_attn<<<B * H * SQ, 64, 0, stream>>>(q, kc, vc, o);
    k_o<<<dim3(D / 128, B * SQ), 128, 0, stream>>>(o, wo, (u16*)d_out);
}
```

```cpp
#include <hip/hip_runtime.h>
#include <stdint.h>

#define LAS __attribute__((address_space(3)))
#define GAS __attribute__((address_space(1)))
typedef unsigned short u16;
typedef short s16x4 __attribute__((ext_vector_type(4)));
typedef short s16x8 __attribute__((ext_vector_type(8)));
typedef float f32x4 __attribute__((ext_vector_type(4)));
typedef unsigned u32x4 __attribute__((ext_vector_type(4)));
typedef unsigned u32x2 __attribute__((ext_vector_type(2)));
typedef GAS unsigned gu32;
#define RLX_AGENT __ATOMIC_RELAXED, __HIP_MEMORY_SCOPE_AGENT
#define DI __device__ __forceinline__

namespace {

constexpr int NB = 8, SQ = 64, DM = 640, NH = 32, SK = 64, DH = 192, HD = NH * DH;
constexpr int AUX_SC1 = 16;
#define AUX_W 0
#define AUX_KV 0
#define AUX_X 0

constexpr int SLOT = 32768;
constexpr int XREG = 4 * SLOT;
constexpr int LDS_BYTES = 163840;

constexpr unsigned WS_E = 0;
constexpr unsigned WS_PF = 4096;
constexpr unsigned WS_OF = WS_PF + 256 * 128;
constexpr unsigned WS_P = 131072;
constexpr unsigned WS_IN = WS_P + 256 * 8192;
constexpr unsigned WS_END = WS_IN + 256 * 8 * 2560;
constexpr unsigned SPIN_CAP = 1u << 18;

constexpr int NITEM = 23;
constexpr int NSTEP = 24;
constexpr int item_cnt(int it) { return it < 10 ? 8 : (it == 10 ? 6 : 7); }
constexpr int need_item(int t) { return t <= 9 ? t : (t == 10 ? -1 : t - 1); }
constexpr int issue_item(int t) {
    return t == 0 ? -1 : (t <= 10 ? t + 3 : (t == 11 ? -1 : (t + 2 < NITEM ? t + 2 : -1)));
}
constexpr int last_issued_before(int t) {
    int last = 3;
    for (int u = 0; u < t; ++u) if (issue_item(u) > last) last = issue_item(u);
    return last;
}
constexpr int wait_cnt(int t) {
    int need = need_item(t);
    if (need < 0) return 63;
    int c = 0;
    for (int i = need + 1; i <= last_issued_before(t); ++i) c += item_cnt(i);
    return c;
}

template <int N> DI void wait_vm() { asm volatile("s_waitcnt vmcnt(%0)" ::"i"(N) : "memory"); }
DI void bar() { asm volatile("s_barrier" ::: "memory"); }
DI void bar_lgkm() { asm volatile("s_waitcnt lgkmcnt(0)\n\ts_barrier" ::: "memory"); }

DI f32x4 mfma(s16x8 a, s16x8 b, f32x4 c) { return __builtin_amdgcn_mfma_f32_16x16x32_bf16(a, b, c, 0, 0, 0); }
DI s16x8 lds_b128(LAS char* p) { return *(LAS s16x8*)p; }
DI s16x8 lds_tr(LAS char* p, int hi_off) {
    s16x4 lo = __builtin_amdgcn_ds_read_tr16_b64_v4i16((LAS s16x4*)p);
    s16x4 hi = __builtin_amdgcn_ds_read_tr16_b64_v4i16((LAS s16x4*)(p + hi_off));
    return __builtin_shufflevector(lo, hi, 0, 1, 2, 3, 4, 5, 6, 7);
}
typedef __bf16 bf16x2 __attribute__((ext_vector_type(2)));
typedef float f32x2 __attribute__((ext_vector_type(2)));
DI unsigned pk2(float a, float b) {
    f32x2 v = {a, b};
    bf16x2 r = __builtin_convertvector(v, bf16x2);
    return __builtin_bit_cast(unsigned, r);
}
DI s16x8 pack8(f32x4 a, f32x4 b) {
    u32x4 w = {pk2(a[0], a[1]), pk2(a[2], a[3]), pk2(b[0], b[1]), pk2(b[2], b[3])};
    return __builtin_bit_cast(s16x8, w);
}
DI float bflo(unsigned w) { return __uint_as_float(w << 16); }
DI float bfhi(unsigned w) { return __uint_as_float(w & 0xffff0000u); }

struct Frags { s16x8 nat[4]; s16x8 tr[3]; };

template <int I, int N, class F> DI void static_for(F&& f) {
    if constexpr (I < N) { f(std::integral_constant<int, I>{}); static_for<I + 1, N>(f); }
}

DI bool poll4(__amdgpu_buffer_rsrc_t rws, unsigned off, unsigned epoch) {
    for (unsigned spin = 0; spin < SPIN_CAP; ++spin) {
        u32x4 v = __builtin_amdgcn_raw_buffer_load_b128(rws, off, 0, AUX_SC1);
        bool ok = v[0] == epoch && v[1] == epoch && v[2] == epoch && v[3] == epoch;
        if (__all(ok)) return true;
        __builtin_amdgcn_s_sleep(1);
    }
    return false;
}

}

__global__ __launch_bounds__(512, 2) void mha_fused(const u16* __restrict__ x, const u16* __restrict__ kc,
                                                    const u16* __restrict__ vc, const u16* __restrict__ wq,
                                                    const u16* __restrict__ wo, u16* __restrict__ out,
                                                    char* __restrict__ ws) {
    __shared__ __attribute__((aligned(16))) char smem_[LDS_BYTES];
    LAS char* smem = (LAS char*)smem_;
    const int bid = blockIdx.x;
    const int g = bid & 7, j = (bid >> 3) & 3, b = bid >> 5;
    const int h = 4 * g + j;
    const int wave = __builtin_amdgcn_readfirstlane(threadIdx.x >> 6);
    const int lane = threadIdx.x & 63;
    const int offA = (((b * 10) >> 3) + ((j * 5) >> 1)) % 10;
    const int hrot = (j + b) & 3;
    const int crot = b >> 2;

    if (wave >= 4) {
        const int lw = wave - 4;
        const __amdgpu_buffer_rsrc_t rx = __builtin_amdgcn_make_buffer_rsrc((void*)x, 0, NB * SQ * DM * 2, 0x00020000);
        const __amdgpu_buffer_rsrc_t rk = __builtin_amdgcn_make_buffer_rsrc((void*)kc, 0, NB * NH * SK * DH * 2, 0x00020000);
        const __amdgpu_buffer_rsrc_t rv = __builtin_amdgcn_make_buffer_rsrc((void*)vc, 0, NB * NH * SK * DH * 2, 0x00020000);
        const __amdgpu_buffer_rsrc_t rq = __builtin_amdgcn_make_buffer_rsrc((void*)wq, 0, DM * HD * 2, 0x00020000);
        const __amdgpu_buffer_rsrc_t ro = __builtin_amdgcn_make_buffer_rsrc((void*)wo, 0, HD * DM * 2, 0x00020000);
        const unsigned sw8 = (unsigned)(((lane & 7) ^ (lane >> 3)) * 16);
        const unsigned vx = (unsigned)((lane >> 3) * (DM * 2)) + sw8;
        const unsigned vkv = (unsigned)((lane >> 3) * (DH * 2)) + sw8;
        unsigned vq[6], vo[5];
#pragma unroll
        for (int i = 0; i < 6; ++i) {
            const int n = 64 * i + lane, r = n / 24, c = n % 24;
            const int s = ((r >> 1) & 1) | (((r >> 3) & 1) << 1);
            vq[i] = (unsigned)(r * (HD * 2) + (2 * ((c >> 1) ^ s) + (c & 1)) * 16);
        }
#pragma unroll
        for (int i = 0; i < 5; ++i) {
            const int n = 64 * i + lane, r = n / 20, c = n % 20;
            vo[i] = (unsigned)(r * (DM * 2) + (2 * ((c >> 1) ^ ((r >> 3) & 1)) + (c & 1)) * 16);
        }
        auto issue = [&](auto IT) {
            constexpr int it = decltype(IT)::value;
            LAS char* sb = smem + (it & 3) * SLOT;
            if constexpr (it < 10) {
                int ks = it + offA; if (ks >= 10) ks -= 10;
#pragma unroll
                for (int p2 = 0; p2 < 2; ++p2) {
                    const int p = 2 * lw + p2;
                    __builtin_amdgcn_raw_ptr_buffer_load_lds(rx, (LAS void*)(sb + p * 1024), 16, vx,
                                                             (unsigned)((b * 64 + 8 * p) * (DM * 2) + ks * 128), 0, AUX_X);
                }
                const unsigned so = (unsigned)((ks * 64 + 16 * lw) * (HD * 2) + h * (DH * 2));
#pragma unroll
                for (int i = 0; i < 6; ++i)
                    __builtin_amdgcn_raw_ptr_buffer_load_lds(rq, (LAS void*)(sb + 8192 + (lw * 6 + i) * 1024), 16, vq[i], so, 0, AUX_W);
            } else if constexpr (it == 10) {
#pragma unroll
                for (int st = 0; st < 3; ++st)
#pragma unroll
                    for (int p2 = 0; p2 < 2; ++p2) {
                        const int p = 2 * lw + p2;
                        __builtin_amdgcn_raw_ptr_buffer_load_lds(rk, (LAS void*)(sb + st * 8192 + p * 1024), 16, vkv,
                                                                 (unsigned)(((b * NH + h) * SK + 8 * p) * (DH * 2) + st * 128), 0, AUX_KV);
                    }
            } else {
                constexpr int s = it - 11, m = s / 3;
                const int hh = 4 * g + ((m + hrot) & 3);
                int ch = (s % 3) + crot; if (ch >= 3) ch -= 3;
#pragma unroll
                for (int p2 = 0; p2 < 2; ++p2) {
                    const int p = 2 * lw + p2;
                    __builtin_amdgcn_raw_ptr_buffer_load_lds(rv, (LAS void*)(sb + p * 1024), 16, vkv,
                                                             (unsigned)(((b * NH + hh) * SK + 8 * p) * (DH * 2) + ch * 128), 0, AUX_KV);
                }
                const unsigned so = (unsigned)((hh * DH + ch * 64 + 16 * lw) * (DM * 2) + j * 320);
#pragma unroll
                for (int i = 0; i < 5; ++i)
                    __builtin_amdgcn_raw_ptr_buffer_load_lds(ro, (LAS void*)(sb + 8192 + (lw * 5 + i) * 1024), 16, vo[i], so, 0, AUX_W);
            }
        };
        static_for<0, 4>(issue);
        static_for<0, NSTEP>([&](auto T) {
            constexpr int t = decltype(T)::value;
            wait_vm<wait_cnt(t)>();
            bar();
            if constexpr (issue_item(t) >= 0) issue(std::integral_constant<int, issue_item(t)>{});
        });
        return;
    }

    const int w = wave;
    const int l15 = lane & 15, lg = lane >> 4;
    const __amdgpu_buffer_rsrc_t rws = __builtin_amdgcn_make_buffer_rsrc((void*)ws, 0, WS_END, 0x00020000);
    const unsigned epoch =
        (unsigned)__builtin_amdgcn_readfirstlane(__hip_atomic_load((gu32*)(ws + WS_E + b * 128), RLX_AGENT)) + 1u;

    const int an0 = l15 * 128 + ((lg ^ (l15 & 7)) << 4);
    const int q4 = l15 >> 2, p4 = l15 & 3;
    const int sq = ((q4 >> 1) & 1) | ((lg & 1) << 1);
    int aq[3], ao[3];
    const int u0 = w < 2 ? 3 * w : 2 * w + 2;
#pragma unroll
    for (int u = 0; u < 3; ++u) {
        aq[u] = 8192 + (8 * lg + q4) * 384 + (((3 * w + u) ^ sq) << 5) + p4 * 8;
        int U = u0 + u; if (w >= 2 && u == 2) U = u0 + 1;
        ao[u] = 8192 + (8 * lg + q4) * 320 + ((U ^ (lg & 1)) << 5) + p4 * 8;
    }

    auto load_A = [&](Frags& f, int slot, int ks) {
        LAS char* sb = smem + slot * SLOT;
#pragma unroll
        for (int u = 0; u < 3; ++u) f.tr[u] = lds_tr(sb + aq[u] + ks * 12288, 1536);
#pragma unroll
        for (int t = 0; t < 4; ++t) f.nat[t] = lds_b128(sb + (an0 ^ (ks << 6)) + t * 2048);
    };
    auto load_B = [&](Frags& f, int slot, int ks) {
        LAS char* sb = smem + slot * SLOT;
#pragma unroll
        for (int u = 0; u < 3; ++u) f.tr[u] = lds_tr(sb + ao[u] + ks * 10240, 1280);
#pragma unroll
        for (int t = 0; t < 4; ++t) f.nat[t] = lds_b128(sb + (an0 ^ (ks << 6)) + t * 2048);
    };

    f32x4 acc[3][4];
#pragma unroll
    for (int u = 0; u < 3; ++u)
#pragma unroll
        for (int t = 0; t < 4; ++t) acc[u][t] = f32x4{0.f, 0.f, 0.f, 0.f};
    auto mma_A = [&](const Frags& f) {
#pragma unroll
        for (int u = 0; u < 3; ++u)
#pragma unroll
            for (int t = 0; t < 4; ++t) acc[u][t] = mfma(f.tr[u], f.nat[t], acc[u][t]);
    };
    auto mma_B = [&](const Frags& f) {
#pragma unroll
        for (int u = 0; u < 3; ++u)
#pragma unroll
            for (int t = 0; t < 4; ++t) acc[u][t] = mfma(f.nat[t], f.tr[u], acc[u][t]);
    };

    Frags f0, f1;
    static_for<0, 10>([&](auto T) {
        constexpr int t = decltype(T)::value;
        bar();
        load_A(f0, t & 3, 0);
        if constexpr (t > 0) mma_A(f1);
        load_A(f1, t & 3, 1);
        mma_A(f0);
        asm volatile("s_waitcnt lgkmcnt(0)" ::: "memory");
    });
    bar();
    mma_A(f1);
#pragma unroll
    for (int u = 0; u < 3; ++u) {
        const int T = 3 * w + u;
#pragma unroll
        for (int t = 0; t < 4; ++t) {
            u32x2 v = {pk2(acc[u][t][0], acc[u][t][1]), pk2(acc[u][t][2], acc[u][t][3])};
            const int chunk = 2 * (T & 3) + (lg >> 1);
            *(LAS u32x2*)(smem + XREG + (T >> 2) * 8192 + (16 * t + l15) * 128 + ((chunk ^ (l15 & 7)) << 4) + (lg & 1) * 8) = v;
        }
    }
    asm volatile("s_waitcnt lgkmcnt(0)" ::: "memory");
    bar();
    {
        f32x4 s[4];
#pragma unroll
        for (int t = 0; t < 4; ++t) s[t] = f32x4{0.f, 0.f, 0.f, 0.f};
        LAS char* kb = smem + (10 & 3) * SLOT;
#pragma unroll
        for (int k6 = 0; k6 < 6; ++k6) {
            const int o = (k6 >> 1) * 8192 + (an0 ^ ((k6 & 1) << 6));
            const s16x8 qf = lds_b128(smem + XREG + o + w * 2048);
#pragma unroll
            for (int t = 0; t < 4; ++t) s[t] = mfma(lds_b128(kb + o + t * 2048), qf, s[t]);
        }
        float mx = s[0][0];
#pragma unroll
        for (int t = 0; t < 4; ++t)
#pragma unroll
            for (int i = 0; i < 4; ++i) mx = fmaxf(mx, s[t][i]);
        mx = fmaxf(mx, __shfl_xor(mx, 16));
        mx = fmaxf(mx, __shfl_xor(mx, 32));
        constexpr float C = 0.07216878364870322f * 1.4426950408889634f;
        const float mc = mx * C;
        float sum = 0.f;
#pragma unroll
        for (int t = 0; t < 4; ++t)
#pragma unroll
            for (int i = 0; i < 4; ++i) { s[t][i] = __builtin_amdgcn_exp2f(fmaf(s[t][i], C, -mc)); sum += s[t][i]; }
        sum += __shfl_xor(sum, 16);
        sum += __shfl_xor(sum, 32);
        const float rs = 1.0f / sum;
#pragma unroll
        for (int t = 0; t < 4; ++t) s[t] *= rs;
#pragma unroll
        for (int s2 = 0; s2 < 2; ++s2) {
            const s16x8 pf = pack8(s[2 * s2], s[2 * s2 + 1]);
            __builtin_amdgcn_raw_buffer_store_b128(__builtin_bit_cast(u32x4, pf), rws,
                                                   WS_P + bid * 8192 + (w * 2 + s2) * 1024 + lane * 16, 0, AUX_SC1);
            *(LAS s16x8*)(smem + XREG + 24576 + (w * 2 + s2) * 1024 + lane * 16) = pf;
        }
    }
#pragma unroll
    for (int u = 0; u < 3; ++u)
#pragma unroll
        for (int t = 0; t < 4; ++t) acc[u][t] = f32x4{0.f, 0.f, 0.f, 0.f};
    asm volatile("s_waitcnt lgkmcnt(0)" ::: "memory");

    f32x4 oacc[3][4];
#pragma unroll
    for (int u = 0; u < 3; ++u)
#pragma unroll
        for (int t = 0; t < 4; ++t) oacc[u][t] = f32x4{0.f, 0.f, 0.f, 0.f};
    s16x8 vw[4][3][2];
    u32x4 pin[8];
    auto cvt_vw = [&](auto M) {
        constexpr int m = decltype(M)::value;
#pragma unroll
        for (int u = 0; u < 3; ++u) {
            vw[m][u][0] = pack8(acc[u][0], acc[u][1]);
            vw[m][u][1] = pack8(acc[u][2], acc[u][3]);
#pragma unroll
            for (int t = 0; t < 4; ++t) acc[u][t] = f32x4{0.f, 0.f, 0.f, 0.f};
        }
    };
    auto do_C = [&](auto M, auto S2) {
        constexpr int m = decltype(M)::value, s2 = decltype(S2)::value;
        const int sidx = ((m + hrot) - j + 3) & 3;
        LAS char* pb = smem + XREG + sidx * 8192 + s2 * 1024 + lane * 16;
#pragma unroll
        for (int t = 0; t < 4; ++t) {
            const s16x8 pf = lds_b128(pb + t * 2048);
#pragma unroll
            for (int u = 0; u < 3; ++u) oacc[u][t] = mfma(vw[m][u][s2], pf, oacc[u][t]);
        }
    };
    using I0 = std::integral_constant<int, 0>;
    using I1 = std::integral_constant<int, 1>;
    static_for<0, 12>([&](auto S) {
        constexpr int s = decltype(S)::value;
        constexpr int slot = (11 + s) & 3;
        bar();
        load_B(f0, slot, 0);
        if constexpr (s > 0) mma_B(f1);
        if constexpr (s > 0 && s % 3 == 0) cvt_vw(std::integral_constant<int, s / 3 - 1>{});
        load_B(f1, slot, 1);
        mma_B(f0);
        if constexpr (s == 1) {
            wait_vm<0>();
            if (lane == 0) __hip_atomic_store((gu32*)(ws + WS_PF + bid * 128 + w * 4), epoch, RLX_AGENT);
        }
        if constexpr (s == 4) {
            if (w > 0) {
                const int src = (bid & ~24) | (((j + w) & 3) << 3);
                poll4(rws, WS_PF + src * 128, epoch);
                __builtin_amdgcn_fence(__ATOMIC_ACQUIRE, "wavefront");
#pragma unroll
                for (int f = 0; f < 8; ++f)
                    pin[f] = __builtin_amdgcn_raw_buffer_load_b128(rws, WS_P + src * 8192 + f * 1024 + lane * 16, 0, AUX_SC1);
            }
        }
        if constexpr (s == 6) {
            if (w > 0) {
#pragma unroll
                for (int f = 0; f < 8; ++f) *(LAS u32x4*)(smem + XREG + (w - 1) * 8192 + f * 1024 + lane * 16) = pin[f];
            }
        }
        if constexpr (s == 7) do_C(I0{}, I0{});
        if constexpr (s == 8) { do_C(I0{}, I1{}); do_C(I1{}, I0{}); }
        if constexpr (s == 9) do_C(I1{}, I1{});
        if constexpr (s == 10) do_C(std::integral_constant<int, 2>{}, I0{});
        if constexpr (s == 11) do_C(std::integral_constant<int, 2>{}, I1{});
        asm volatile("s_waitcnt lgkmcnt(0)" ::: "memory");
    });
    mma_B(f1);
    cvt_vw(std::integral_constant<int, 3>{});
    do_C(std::integral_constant<int, 3>{}, I0{});
    do_C(std::integral_constant<int, 3>{}, I1{});

    {
        const int nt = w < 2 ? 3 : 2;
#pragma unroll
        for (int u = 0; u < 3; ++u) {
            if (u < nt) {
#pragma unroll
                for (int m = 0; m < 2; ++m) {
                    const s16x8 v = pack8(oacc[u][2 * m], oacc[u][2 * m + 1]);
                    const int e = 4 * m + (l15 >> 2);
                    const int dst = (bid & ~7) | e;
                    __builtin_amdgcn_raw_buffer_store_b128(__builtin_bit_cast(u32x4, v), rws,
                                                           WS_IN + (dst * 8 + g) * 2560 + (u0 + u) * 256 + (lg * 4 + (l15 & 3)) * 16, 0, AUX_SC1);
                }
            }
        }
        wait_vm<0>();
        if (lane == 0) __hip_atomic_store((gu32*)(ws + WS_OF + bid * 128 + w * 4), epoch, RLX_AGENT);
        if (w == 0) {
            poll4(rws, WS_OF + ((bid & ~7) | (lane & 7)) * 128, epoch);
            __builtin_amdgcn_fence(__ATOMIC_ACQUIRE, "wavefront");
        }
        bar();
        const int T = w * 64 + lane;
        if (T < 160) {
            u32x4 v[8];
#pragma unroll
            for (int s = 0; s < 8; ++s)
                v[s] = __builtin_amdgcn_raw_buffer_load_b128(rws, WS_IN + (bid * 8 + s) * 2560 + T * 16, 0, AUX_SC1);
            float r[8];
#pragma unroll
            for (int i = 0; i < 8; ++i) r[i] = 0.f;
#pragma unroll
            for (int s = 0; s < 8; ++s)
#pragma unroll
                for (int i = 0; i < 4; ++i) { r[2 * i] += bflo(v[s][i]); r[2 * i + 1] += bfhi(v[s][i]); }
            const int U = T >> 4, gg = (T >> 2) & 3, qq = T & 3;
            const int row = 32 * (g >> 2) + 4 * (g & 3) + qq;
            u16* o = out + (b * SQ + row) * DM + j * 160 + 16 * U + 4 * gg;
            *(u32x2*)o = u32x2{pk2(r[0], r[1]), pk2(r[2], r[3])};
            *(u32x2*)(o + 16 * DM) = u32x2{pk2(r[4], r[5]), pk2(r[6], r[7])};
        }
        if (g == 0 && j == 0 && threadIdx.x == 0) __hip_atomic_store((gu32*)(ws + WS_E + b * 128), epoch, RLX_AGENT);
    }
}

extern "C" void kernel_launch(void* const* d_in, const int* in_sizes, int n_in, void* d_out, int out_size,
                              void* d_ws, size_t ws_size, hipStream_t stream) {
    mha_fused<<<256, 512, 0, stream>>>((const u16*)d_in[0], (const u16*)d_in[1], (const u16*)d_in[2],
                                       (const u16*)d_in[3], (const u16*)d_in[4], (u16*)d_out, (char*)d_ws);
}
```

```cpp
#include <hip/hip_runtime.h>
#include <stdint.h>
#include <type_traits>

#define LAS __attribute__((address_space(3)))
#define GAS __attribute__((address_space(1)))
typedef unsigned short u16;
typedef short s16x4 __attribute__((ext_vector_type(4)));
typedef short s16x8 __attribute__((ext_vector_type(8)));
typedef float f32x4 __attribute__((ext_vector_type(4)));
typedef unsigned u32x4 __attribute__((ext_vector_type(4)));
typedef unsigned u32x2 __attribute__((ext_vector_type(2)));
typedef GAS unsigned gu32;
#define RLX_AGENT __ATOMIC_RELAXED, __HIP_MEMORY_SCOPE_AGENT
#define DI __device__ __forceinline__

namespace {

constexpr int NB = 8, SQ = 64, DM = 640, NH = 32, SK = 64, DH = 192, HD = NH * DH;
constexpr int AUX_SC1 = 16;
#define AUX_W 0
#define AUX_KV 0
#define AUX_X 0

constexpr int SLOT = 32768;
constexpr int XREG = 4 * SLOT;
constexpr int LDS_BYTES = 163840;

constexpr unsigned WS_E = 0;
constexpr unsigned WS_PF = 4096;
constexpr unsigned WS_OF = WS_PF + 256 * 128;
constexpr unsigned WS_P = 131072;
constexpr unsigned WS_IN = WS_P + 256 * 8192;
constexpr unsigned WS_END = WS_IN + 256 * 8 * 2560;
constexpr unsigned SPIN_CAP = 1u << 18;
constexpr unsigned VOOB = 0x80000000u;

constexpr int NITEM = 23;
constexpr int NSTEP = 24;
constexpr int item_cnt(int it) { return it < 10 ? 8 : (it == 10 ? 6 : 7); }
constexpr int need_item(int t) { return t <= 9 ? t : (t == 10 ? -1 : t - 1); }
constexpr int issue_item(int t) {
    return t == 0 ? -1 : (t <= 10 ? t + 3 : (t == 11 ? -1 : (t + 2 < NITEM ? t + 2 : -1)));
}
constexpr int last_issued_before(int t) {
    int last = 3;
    for (int u = 0; u < t; ++u) if (issue_item(u) > last) last = issue_item(u);
    return last;
}
constexpr int wait_cnt(int t) {
    int need = need_item(t);
    if (need < 0) return 63;
    int c = 0;
    for (int i = need + 1; i <= last_issued_before(t); ++i) c += item_cnt(i);
    return c;
}

#define STAMP(i)
#define ACC_BEGIN()
#define ACC(i) do {} while (0)
#define STAMP_DECL
#define STAMP_OUT(role)
template <int N> DI void wait_vm() { asm volatile("s_waitcnt vmcnt(%0)" ::"i"(N) : "memory"); }
DI void bar() { asm volatile("s_barrier" ::: "memory"); }
#define SB() __builtin_amdgcn_sched_barrier(0)
DI void wait_lgkm0() { __builtin_amdgcn_s_waitcnt(0xC07F); asm volatile("" ::: "memory"); }
DI void bar_lgkm() { asm volatile("s_waitcnt lgkmcnt(0)\n\ts_barrier" ::: "memory"); }

DI f32x4 mfma(u32x4 a, u32x4 b, f32x4 c) { return __builtin_amdgcn_mfma_f32_16x16x32_bf16(__builtin_bit_cast(s16x8, a), __builtin_bit_cast(s16x8, b), c, 0, 0, 0); }
DI u32x4 lds_b128(LAS char* p) { return *(LAS u32x4*)p; }
DI u32x4 lds_tr(LAS char* p, int hi_off) {
    u32x2 lo = __builtin_bit_cast(u32x2, __builtin_amdgcn_ds_read_tr16_b64_v4i16((LAS s16x4*)p));
    u32x2 hi = __builtin_bit_cast(u32x2, __builtin_amdgcn_ds_read_tr16_b64_v4i16((LAS s16x4*)(p + hi_off)));
    return u32x4{lo[0], lo[1], hi[0], hi[1]};
}
typedef __bf16 bf16x2 __attribute__((ext_vector_type(2)));
typedef float f32x2 __attribute__((ext_vector_type(2)));
DI unsigned pk2(float a, float b) {
    f32x2 v = {a, b};
    bf16x2 r = __builtin_convertvector(v, bf16x2);
    return __builtin_bit_cast(unsigned, r);
}
DI u32x4 pack8(f32x4 a, f32x4 b) {
    return u32x4{pk2(a[0], a[1]), pk2(a[2], a[3]), pk2(b[0], b[1]), pk2(b[2], b[3])};
}
DI float bflo(unsigned w) { return __uint_as_float(w << 16); }
DI float bfhi(unsigned w) { return __uint_as_float(w & 0xffff0000u); }

struct Frags { u32x4 nat[4]; u32x4 tr[3]; };

template <int I, int N, class F> DI void static_for(F&& f) {
    if constexpr (I < N) { f(std::integral_constant<int, I>{}); static_for<I + 1, N>(f); }
}

DI void dma_p(__amdgpu_buffer_rsrc_t r, unsigned voff, unsigned lds_dst) {
    unsigned keep;
    asm volatile(
        "s_nop 4\n\ts_mov_b32 %0, m0\n\t"
        "s_mov_b32 m0, %3\n\ts_nop 0\n\t"
        "buffer_load_dwordx4 %1, %2, 0 offen sc1 lds\n\t"
        "buffer_load_dwordx4 %1, %2, 0 offen offset:1024 sc1 lds\n\t"
        "buffer_load_dwordx4 %1, %2, 0 offen offset:2048 sc1 lds\n\t"
        "buffer_load_dwordx4 %1, %2, 0 offen offset:3072 sc1 lds\n\t"
        "s_add_u32 m0, m0, 0x1000\n\ts_nop 0\n\t"
        "buffer_load_dwordx4 %1, %2, %4 offen sc1 lds\n\t"
        "buffer_load_dwordx4 %1, %2, %4 offen offset:1024 sc1 lds\n\t"
        "buffer_load_dwordx4 %1, %2, %4 offen offset:2048 sc1 lds\n\t"
        "buffer_load_dwordx4 %1, %2, %4 offen offset:3072 sc1 lds\n\t"
        "s_mov_b32 m0, %0"
        : "=&s"(keep) : "v"(voff), "s"(r), "s"(lds_dst), "s"(4096u) : "memory", "scc");
}

DI bool poll4(__amdgpu_buffer_rsrc_t rws, unsigned off, unsigned epoch) {
#pragma unroll 1
    for (unsigned spin = 0; spin < SPIN_CAP; ++spin) {
        u32x4 v = __builtin_amdgcn_raw_buffer_load_b128(rws, off, 0, AUX_SC1);
        const unsigned d = (v[0] ^ epoch) | (v[1] ^ epoch) | (v[2] ^ epoch) | (v[3] ^ epoch);
        if (__all(d == 0)) return true;
        __builtin_amdgcn_s_sleep(1);
    }
    return false;
}

}

__global__ __launch_bounds__(512, 2) void mha_fused(const u16* __restrict__ x, const u16* __restrict__ kc,
                                                    const u16* __restrict__ vc, const u16* __restrict__ wq,
                                                    const u16* __restrict__ wo, u16* __restrict__ out,
                                                    char* __restrict__ ws) {
    __shared__ __attribute__((aligned(16))) char smem_[LDS_BYTES];
    LAS char* smem = (LAS char*)smem_;
    const int bid = blockIdx.x;
    const int g = bid & 7, j = (bid >> 3) & 3, b = bid >> 5;
    const int h = 4 * g + j;
    const int wave = __builtin_amdgcn_readfirstlane(threadIdx.x >> 6);
    const int lane = threadIdx.x & 63;
    const int offA = (((b * 10) >> 3) + ((j * 5) >> 1)) % 10;
    const int hrot = (j + b) & 3;
    const int crot = b >> 2;

    if (wave >= 4) {
        const int lw = wave - 4;
        const __amdgpu_buffer_rsrc_t rx = __builtin_amdgcn_make_buffer_rsrc((void*)x, 0, NB * SQ * DM * 2, 0x00020000);
        const __amdgpu_buffer_rsrc_t rk = __builtin_amdgcn_make_buffer_rsrc((void*)kc, 0, NB * NH * SK * DH * 2, 0x00020000);
        const __amdgpu_buffer_rsrc_t rv = __builtin_amdgcn_make_buffer_rsrc((void*)vc, 0, NB * NH * SK * DH * 2, 0x00020000);
        const __amdgpu_buffer_rsrc_t rq = __builtin_amdgcn_make_buffer_rsrc((void*)wq, 0, DM * HD * 2, 0x00020000);
        const __amdgpu_buffer_rsrc_t ro = __builtin_amdgcn_make_buffer_rsrc((void*)wo, 0, HD * DM * 2, 0x00020000);
        const unsigned sw8 = (unsigned)(((lane & 7) ^ (lane >> 3)) * 16);
        const unsigned vx = (unsigned)((lane >> 3) * (DM * 2)) + sw8;
        const unsigned vkv = (unsigned)((lane >> 3) * (DH * 2)) + sw8;
        unsigned vq[6], vo[5];
#pragma unroll
        for (int i = 0; i < 6; ++i) {
            const int n = 64 * i + lane, r = n / 24, c = n % 24;
            const int s = ((r >> 1) & 1) | (((r >> 3) & 1) << 1);
            vq[i] = (unsigned)(r * (HD * 2) + (2 * ((c >> 1) ^ s) + (c & 1)) * 16);
        }
#pragma unroll
        for (int i = 0; i < 5; ++i) {
            const int n = 64 * i + lane, r = n / 20, c = n % 20;
            vo[i] = (unsigned)(r * (DM * 2) + (2 * ((c >> 1) ^ ((r >> 3) & 1)) + (c & 1)) * 16);
        }
        auto issue = [&](int it) {
            LAS char* sb = smem + (it & 3) * SLOT;
            if (it < 10) {
                int ks = it + offA; if (ks >= 10) ks -= 10;
                const unsigned sx = (unsigned)((b * 64 + 16 * lw) * (DM * 2) + ks * 128);
                __builtin_amdgcn_raw_ptr_buffer_load_lds(rx, (LAS void*)(sb + lw * 2048), 16, vx, sx, 0, AUX_X);
                __builtin_amdgcn_raw_ptr_buffer_load_lds(rx, (LAS void*)(sb + lw * 2048 + 1024), 16, vx, sx + 8 * DM * 2, 0, AUX_X);
                const unsigned so = (unsigned)((ks * 64 + 16 * lw) * (HD * 2) + h * (DH * 2));
#pragma unroll
                for (int i = 0; i < 6; ++i)
                    __builtin_amdgcn_raw_ptr_buffer_load_lds(rq, (LAS void*)(sb + 8192 + lw * 6144 + i * 1024), 16, vq[i], so, 0, AUX_W);
            } else if (it == 10) {
                const unsigned sk = (unsigned)(((b * NH + h) * SK + 16 * lw) * (DH * 2));
#pragma unroll
                for (int st = 0; st < 3; ++st) {
                    __builtin_amdgcn_raw_ptr_buffer_load_lds(rk, (LAS void*)(sb + st * 8192 + lw * 2048), 16, vkv, sk + st * 128, 0, AUX_KV);
                    __builtin_amdgcn_raw_ptr_buffer_load_lds(rk, (LAS void*)(sb + st * 8192 + lw * 2048 + 1024), 16, vkv, sk + st * 128 + 8 * DH * 2, 0, AUX_KV);
                }
                __builtin_amdgcn_raw_ptr_buffer_load_lds(rk, (LAS void*)(sb + 24576 + lw * 2048), 16, VOOB, 0, 0, 0);
                __builtin_amdgcn_raw_ptr_buffer_load_lds(rk, (LAS void*)(sb + 24576 + lw * 2048 + 1024), 16, VOOB, 0, 0, 0);
            } else {
                const int s = it - 11, m = (s * 11) >> 5;
                const int hh = 4 * g + ((m + hrot) & 3);
                int ch = (s - 3 * m) + crot; if (ch >= 3) ch -= 3;
                const unsigned vkv2 = it < NITEM ? vkv : VOOB;
                const unsigned sv = (unsigned)(((b * NH + hh) * SK + 16 * lw) * (DH * 2) + ch * 128);
                __builtin_amdgcn_raw_ptr_buffer_load_lds(rv, (LAS void*)(sb + lw * 2048), 16, vkv2, sv, 0, AUX_KV);
                __builtin_amdgcn_raw_ptr_buffer_load_lds(rv, (LAS void*)(sb + lw * 2048 + 1024), 16, vkv2, sv + 8 * DH * 2, 0, AUX_KV);
                const unsigned so = it < NITEM ? (unsigned)((hh * DH + ch * 64 + 16 * lw) * (DM * 2) + j * 320) : VOOB;
#pragma unroll
                for (int i = 0; i < 5; ++i)
                    __builtin_amdgcn_raw_ptr_buffer_load_lds(ro, (LAS void*)(sb + 8192 + lw * 5120 + i * 1024), 16, vo[i], so, 0, AUX_W);
                __builtin_amdgcn_raw_ptr_buffer_load_lds(ro, (LAS void*)(sb + 28672 + lw * 1024), 16, VOOB, 0, 0, 0);
            }
        };
        STAMP_DECL
        STAMP(0);
#pragma unroll 1
        for (int it = 0; it < 3; ++it) issue(it);
        STAMP(1);
        int nxt = 3;
        ACC_BEGIN();
#pragma unroll 1
        for (int t = 0; t < NSTEP; ++t) {
            if (t == 11) wait_vm<24>(); else if (t != 10) wait_vm<16>();
            if (t < 10) ACC(10); else ACC(13);
            bar();
            if (t < 10) ACC(11); else ACC(14);
            if (t != 11) { issue(nxt); ++nxt; }
            if (t < 10) ACC(12); else ACC(15);
        }
        STAMP(2);
        STAMP_OUT(1);
        return;
    }

    const int w = wave;
    const int l15 = lane & 15, lg = lane >> 4;
    const __amdgpu_buffer_rsrc_t rws = __builtin_amdgcn_make_buffer_rsrc((void*)ws, 0, WS_END, 0x00020000);
    const unsigned epoch =
        (unsigned)__builtin_amdgcn_readfirstlane(__hip_atomic_load((gu32*)(ws + WS_E + b * 128), RLX_AGENT)) + 1u;

    const int an0 = l15 * 128 + ((lg ^ (l15 & 7)) << 4);
    const int q4 = l15 >> 2, p4 = l15 & 3;
    const int sq = ((q4 >> 1) & 1) | ((lg & 1) << 1);
    int aq[3], ao[3];
    const int u0 = w < 2 ? 3 * w : 2 * w + 2;
#pragma unroll
    for (int u = 0; u < 3; ++u) {
        aq[u] = 8192 + (8 * lg + q4) * 384 + (((3 * w + u) ^ sq) << 5) + p4 * 8;
        int U = u0 + u; if (w >= 2 && u == 2) U = u0 + 1;
        ao[u] = 8192 + (8 * lg + q4) * 320 + ((U ^ (lg & 1)) << 5) + p4 * 8;
    }

    auto load_A = [&](Frags& f, int slot, int ks) {
        LAS char* sb = smem + slot * SLOT;
#pragma unroll
        for (int u = 0; u < 3; ++u) f.tr[u] = lds_tr(sb + aq[u] + ks * 12288, 1536);
#pragma unroll
        for (int t = 0; t < 4; ++t) f.nat[t] = lds_b128(sb + (an0 ^ (ks << 6)) + t * 2048);
    };
    auto load_B = [&](Frags& f, int slot, int ks) {
        LAS char* sb = smem + slot * SLOT;
#pragma unroll
        for (int u = 0; u < 3; ++u) f.tr[u] = lds_tr(sb + ao[u] + ks * 10240, 1280);
#pragma unroll
        for (int t = 0; t < 4; ++t) f.nat[t] = lds_b128(sb + (an0 ^ (ks << 6)) + t * 2048);
    };

    f32x4 acc[3][4];
#pragma unroll
    for (int u = 0; u < 3; ++u)
#pragma unroll
        for (int t = 0; t < 4; ++t) acc[u][t] = f32x4{0.f, 0.f, 0.f, 0.f};
    auto mma_A = [&](const Frags& f) {
#pragma unroll
        for (int u = 0; u < 3; ++u)
#pragma unroll
            for (int t = 0; t < 4; ++t) acc[u][t] = mfma(f.tr[u], f.nat[t], acc[u][t]);
    };
    auto mma_B = [&](const Frags& f) {
#pragma unroll
        for (int u = 0; u < 3; ++u)
#pragma unroll
            for (int t = 0; t < 4; ++t) acc[u][t] = mfma(f.nat[t], f.tr[u], acc[u][t]);
    };

    Frags f0, f1;
#pragma unroll
    for (int t = 0; t < 4; ++t) f1.nat[t] = u32x4{0, 0, 0, 0};
#pragma unroll
    for (int u = 0; u < 3; ++u) f1.tr[u] = u32x4{0, 0, 0, 0};
    STAMP_DECL
    STAMP(0);
#pragma unroll 1
    for (int t = 0; t < 10; ++t) {
        ACC_BEGIN();
        bar();
        ACC(10);
        SB();
        load_A(f0, t & 3, 0);
        SB();
        mma_A(f1);
        SB();
        wait_lgkm0();
        load_A(f1, t & 3, 1);
        SB();
        mma_A(f0);
        SB();
        wait_lgkm0();
        ACC(11);
    }
    STAMP(1);
    bar();
    mma_A(f1);
#pragma unroll
    for (int u = 0; u < 3; ++u) {
        const int T = 3 * w + u;
#pragma unroll
        for (int t = 0; t < 4; ++t) {
            u32x2 v = {pk2(acc[u][t][0], acc[u][t][1]), pk2(acc[u][t][2], acc[u][t][3])};
            const int chunk = 2 * (T & 3) + (lg >> 1);
            *(LAS u32x2*)(smem + XREG + (T >> 2) * 8192 + (16 * t + l15) * 128 + ((chunk ^ (l15 & 7)) << 4) + (lg & 1) * 8) = v;
        }
    }
    asm volatile("s_waitcnt lgkmcnt(0)" ::: "memory");
    bar();
    {
        f32x4 s[4];
#pragma unroll
        for (int t = 0; t < 4; ++t) s[t] = f32x4{0.f, 0.f, 0.f, 0.f};
        LAS char* kb = smem + (10 & 3) * SLOT;
#pragma unroll
        for (int k6 = 0; k6 < 6; ++k6) {
            const int o = (k6 >> 1) * 8192 + (an0 ^ ((k6 & 1) << 6));
            const u32x4 qf = lds_b128(smem + XREG + o + w * 2048);
#pragma unroll
            for (int t = 0; t < 4; ++t) s[t] = mfma(lds_b128(kb + o + t * 2048), qf, s[t]);
        }
        float mx = s[0][0];
#pragma unroll
        for (int t = 0; t < 4; ++t)
#pragma unroll
            for (int i = 0; i < 4; ++i) mx = fmaxf(mx, s[t][i]);
        mx = fmaxf(mx, __shfl_xor(mx, 16));
        mx = fmaxf(mx, __shfl_xor(mx, 32));
        constexpr float C = 0.07216878364870322f * 1.4426950408889634f;
        const float mc = mx * C;
        float sum = 0.f;
#pragma unroll
        for (int t = 0; t < 4; ++t)
#pragma unroll
            for (int i = 0; i < 4; ++i) { s[t][i] = __builtin_amdgcn_exp2f(fmaf(s[t][i], C, -mc)); sum += s[t][i]; }
        sum += __shfl_xor(sum, 16);
        sum += __shfl_xor(sum, 32);
        const float rs = 1.0f / sum;
#pragma unroll
        for (int t = 0; t < 4; ++t) s[t] *= rs;
#pragma unroll
        for (int s2 = 0; s2 < 2; ++s2) {
            const u32x4 pf = pack8(s[2 * s2], s[2 * s2 + 1]);
            __builtin_amdgcn_raw_buffer_store_b128(pf, rws,
                                                   WS_P + bid * 8192 + (w * 2 + s2) * 1024 + lane * 16, 0, AUX_SC1);
            *(LAS u32x4*)(smem + XREG + 24576 + (w * 2 + s2) * 1024 + lane * 16) = pf;
        }
    }
#pragma unroll
    for (int u = 0; u < 3; ++u)
#pragma unroll
        for (int t = 0; t < 4; ++t) acc[u][t] = f32x4{0.f, 0.f, 0.f, 0.f};
    asm volatile("s_waitcnt lgkmcnt(0)" ::: "memory");

    STAMP(2);
    f32x4 oacc[3][4];
#pragma unroll
    for (int u = 0; u < 3; ++u)
#pragma unroll
        for (int t = 0; t < 4; ++t) oacc[u][t] = f32x4{0.f, 0.f, 0.f, 0.f};
    u32x4 vwOld[3][2], vwMid[3][2];
    auto cvt_vw = [&](u32x4 (&vw)[3][2]) {
#pragma unroll
        for (int u = 0; u < 3; ++u) {
            vw[u][0] = pack8(acc[u][0], acc[u][1]);
            vw[u][1] = pack8(acc[u][2], acc[u][3]);
#pragma unroll
            for (int t = 0; t < 4; ++t) acc[u][t] = f32x4{0.f, 0.f, 0.f, 0.f};
        }
    };
    auto do_C = [&](int m, int s2, const u32x4 (&vw)[3][2]) {
        const int sidx = ((m + hrot) - j + 3) & 3;
        LAS char* pb = smem + XREG + sidx * 8192 + lane * 16 + s2 * 1024;
        u32x4 pf[4];
#pragma unroll
        for (int t = 0; t < 4; ++t) pf[t] = lds_b128(pb + t * 2048);
#pragma unroll
        for (int t = 0; t < 4; ++t)
#pragma unroll
            for (int u = 0; u < 3; ++u) oacc[u][t] = mfma(vw[u][s2], pf[t], oacc[u][t]);
    };
#pragma unroll
    for (int t = 0; t < 4; ++t) f1.nat[t] = u32x4{0, 0, 0, 0};
#pragma unroll
    for (int u = 0; u < 3; ++u) {
        f1.tr[u] = u32x4{0, 0, 0, 0};
        vwMid[u][0] = vwMid[u][1] = vwOld[u][0] = vwOld[u][1] = u32x4{0, 0, 0, 0};
    }
#pragma unroll 1
    for (int m = 0; m < 4; ++m) {
        const int slot0 = (11 + 3 * m) & 3;
        bar();
        SB();
        load_B(f0, slot0, 0);
        SB();
        mma_B(f1);
        SB();
        wait_lgkm0();
#pragma unroll
        for (int u = 0; u < 3; ++u) { vwOld[u][0] = vwMid[u][0]; vwOld[u][1] = vwMid[u][1]; }
        load_B(f1, slot0, 1);
        SB();
        cvt_vw(vwMid);
        SB();
        mma_B(f0);
        SB();
        if (m == 2) wait_vm<0>();
        if (m == 3) do_C(1, 0, vwOld);
        wait_lgkm0();
        bar();
        SB();
        load_B(f0, (slot0 + 1) & 3, 0);
        SB();
        mma_B(f1);
        SB();
        wait_lgkm0();
        load_B(f1, (slot0 + 1) & 3, 1);
        SB();
        mma_B(f0);
        SB();
        if (m == 0) {
            wait_vm<0>();
            if (lane == 0) __hip_atomic_store((gu32*)(ws + WS_PF + bid * 128 + w * 4), epoch, RLX_AGENT);
        }
        if (m == 1 && w > 0) {
            const int src = (bid & ~24) | (((j + w) & 3) << 3);
            poll4(rws, WS_PF + src * 128, epoch);
            __builtin_amdgcn_fence(__ATOMIC_ACQUIRE, "wavefront");
            dma_p(rws, WS_P + src * 8192 + lane * 16, (unsigned)(XREG + (w - 1) * 8192));
        }
        if (m == 2) do_C(0, 0, vwOld);
        if (m == 3) { do_C(1, 1, vwOld); do_C(2, 0, vwMid); }
        wait_lgkm0();
        bar();
        SB();
        load_B(f0, (slot0 + 2) & 3, 0);
        SB();
        mma_B(f1);
        SB();
        wait_lgkm0();
        load_B(f1, (slot0 + 2) & 3, 1);
        SB();
        mma_B(f0);
        SB();
        if (m == 2) do_C(0, 1, vwOld);
        if (m == 3) do_C(2, 1, vwMid);
        wait_lgkm0();
    }
    STAMP(3);
    mma_B(f1);
    cvt_vw(vwMid);
    do_C(3, 0, vwMid);
    do_C(3, 1, vwMid);
    STAMP(4);

    {
        const int nt = w < 2 ? 3 : 2;
#pragma unroll
        for (int u = 0; u < 3; ++u) {
            if (u < nt) {
#pragma unroll
                for (int m = 0; m < 2; ++m) {
                    const u32x4 v = pack8(oacc[u][2 * m], oacc[u][2 * m + 1]);
                    const int e = 4 * m + (l15 >> 2);
                    const int dst = (bid & ~7) | e;
                    __builtin_amdgcn_raw_buffer_store_b128(v, rws,
                                                           WS_IN + (dst * 8 + g) * 2560 + (u0 + u) * 256 + (lg * 4 + (l15 & 3)) * 16, 0, AUX_SC1);
                }
            }
        }
        STAMP(5);
        wait_vm<0>();
        STAMP(6);
        if (lane == 0) __hip_atomic_store((gu32*)(ws + WS_OF + bid * 128 + w * 4), epoch, RLX_AGENT);
        if (w == 0) {
            poll4(rws, WS_OF + ((bid & ~7) | (lane & 7)) * 128, epoch);
            __builtin_amdgcn_fence(__ATOMIC_ACQUIRE, "wavefront");
        }
        STAMP(7);
        bar();
        const int T = w * 64 + lane;
        if (T < 160) {
            u32x4 v[8];
#pragma unroll
            for (int s = 0; s < 8; ++s)
                v[s] = __builtin_amdgcn_raw_buffer_load_b128(rws, WS_IN + (bid * 8 + s) * 2560 + T * 16, 0, AUX_SC1);
            float r[8];
#pragma unroll
            for (int i = 0; i < 8; ++i) r[i] = 0.f;
#pragma unroll
            for (int s = 0; s < 8; ++s)
#pragma unroll
                for (int i = 0; i < 4; ++i) { r[2 * i] += bflo(v[s][i]); r[2 * i + 1] += bfhi(v[s][i]); }
            const int U = T >> 4, gg = (T >> 2) & 3, qq = T & 3;
            const int row = 32 * (g >> 2) + 4 * (g & 3) + qq;
            u16* o = out + (b * SQ + row) * DM + j * 160 + 16 * U + 4 * gg;
            *(u32x2*)o = u32x2{pk2(r[0], r[1]), pk2(r[2], r[3])};
            *(u32x2*)(o + 16 * DM) = u32x2{pk2(r[4], r[5]), pk2(r[6], r[7])};
        }
        STAMP(8);
        STAMP(9);
        STAMP_OUT(0);
        if (g == 0 && j == 0 && threadIdx.x == 0) __hip_atomic_store((gu32*)(ws + WS_E + b * 128), epoch, RLX_AGENT);
    }
}

extern "C" void kernel_launch(void* const* d_in, const int* in_sizes, int n_in, void* d_out, int out_size,
                              void* d_ws, size_t ws_size, hipStream_t stream) {
    mha_fused<<<256, 512, 0, stream>>>((const u16*)d_in[0], (const u16*)d_in[1], (const u16*)d_in[2],
                                       (const u16*)d_in[3], (const u16*)d_in[4], (u16*)d_out, (char*)d_ws);
}
```

```cpp
#include <hip/hip_runtime.h>
#include <stdint.h>

#define LAS __attribute__((address_space(3)))
#define GAS __attribute__((address_space(1)))
typedef unsigned short u16;
typedef short s16x4 __attribute__((ext_vector_type(4)));
typedef short s16x8 __attribute__((ext_vector_type(8)));
typedef float f32x4 __attribute__((ext_vector_type(4)));
typedef float f32x2 __attribute__((ext_vector_type(2)));
typedef __bf16 bf16x2 __attribute__((ext_vector_type(2)));
typedef unsigned u32x4 __attribute__((ext_vector_type(4)));
typedef unsigned u32x2 __attribute__((ext_vector_type(2)));
typedef GAS unsigned gu32;
#define RLX_AGENT __ATOMIC_RELAXED, __HIP_MEMORY_SCOPE_AGENT
#define DI __device__ __forceinline__

namespace {

constexpr int NB = 8, SQ = 64, DM = 640, NH = 32, SK = 64, DH = 192, HD = NH * DH;
constexpr int AUX_SC1 = 16;
#define AUX_W 0
#define AUX_KV 0
#define AUX_X 0
#define CODE_PF 7168

constexpr int SLOT = 32768;
constexpr int XREG = 4 * SLOT;
constexpr int LDS_BYTES = 163840;

constexpr unsigned WS_E = 0;
constexpr unsigned WS_PF = 4096;
constexpr unsigned WS_OF = WS_PF + 256 * 128;
constexpr unsigned WS_P = 131072;
constexpr unsigned WS_IN = WS_P + 256 * 8192;
constexpr unsigned WS_END = WS_IN + 256 * 8 * 2560;
constexpr unsigned SPIN_CAP = 1u << 18;
constexpr unsigned VOOB = 0x80000000u;
constexpr int NITEM = 23;

#define STAMP(i)
#define STAMP_DECL
#define STAMP_OUT()
#define TR_DECL
#define TR()
#define TR_OUT()

template <int N> DI void wait_vm() { asm volatile("s_waitcnt vmcnt(%0)" ::"i"(N) : "memory"); }
DI void bar() { asm volatile("s_barrier" ::: "memory"); }
#define BARL() do { bar(); TR(); } while (0)
#define SB() __builtin_amdgcn_sched_barrier(0)
#define SGB(mask, n) __builtin_amdgcn_sched_group_barrier(mask, n, 0)
DI void wait_lgkm0() { __builtin_amdgcn_s_waitcnt(0xC07F); asm volatile("" ::: "memory"); }

DI f32x4 mfma(u32x4 a, u32x4 b, f32x4 c) {
    return __builtin_amdgcn_mfma_f32_16x16x32_bf16(__builtin_bit_cast(s16x8, a), __builtin_bit_cast(s16x8, b), c, 0, 0, 0);
}
DI u32x4 lds_b128(LAS char* p) { return *(LAS u32x4*)p; }
DI u32x4 lds_tr(LAS char* p, int hi_off) {
    u32x2 lo = __builtin_bit_cast(u32x2, __builtin_amdgcn_ds_read_tr16_b64_v4i16((LAS s16x4*)p));
    u32x2 hi = __builtin_bit_cast(u32x2, __builtin_amdgcn_ds_read_tr16_b64_v4i16((LAS s16x4*)(p + hi_off)));
    return u32x4{lo[0], lo[1], hi[0], hi[1]};
}
DI unsigned pk2(float a, float b) {
    f32x2 v = {a, b};
    return __builtin_bit_cast(unsigned, __builtin_convertvector(v, bf16x2));
}
DI u32x4 pack8(f32x4 a, f32x4 b) { return u32x4{pk2(a[0], a[1]), pk2(a[2], a[3]), pk2(b[0], b[1]), pk2(b[2], b[3])}; }
DI float bflo(unsigned w) { return __uint_as_float(w << 16); }
DI float bfhi(unsigned w) { return __uint_as_float(w & 0xffff0000u); }
DI float xmax(float v) {
    auto a = __builtin_amdgcn_permlane16_swap(__float_as_uint(v), __float_as_uint(v), false, false);
    v = fmaxf(__uint_as_float(a[0]), __uint_as_float(a[1]));
    auto c = __builtin_amdgcn_permlane32_swap(__float_as_uint(v), __float_as_uint(v), false, false);
    return fmaxf(__uint_as_float(c[0]), __uint_as_float(c[1]));
}
DI float xsum(float v) {
    auto a = __builtin_amdgcn_permlane16_swap(__float_as_uint(v), __float_as_uint(v), false, false);
    v = __uint_as_float(a[0]) + __uint_as_float(a[1]);
    auto c = __builtin_amdgcn_permlane32_swap(__float_as_uint(v), __float_as_uint(v), false, false);
    return __uint_as_float(c[0]) + __uint_as_float(c[1]);
}

struct Frags { u32x4 nat[4]; u32x4 tr[3]; };

DI void dma_p(__amdgpu_buffer_rsrc_t r, unsigned voff, unsigned lds_dst) {
    unsigned keep;
    asm volatile(
        "s_nop 4\n\ts_mov_b32 %0, m0\n\t"
        "s_mov_b32 m0, %3\n\ts_nop 0\n\t"
        "buffer_load_dwordx4 %1, %2, 0 offen sc1 lds\n\t"
        "buffer_load_dwordx4 %1, %2, 0 offen offset:1024 sc1 lds\n\t"
        "buffer_load_dwordx4 %1, %2, 0 offen offset:2048 sc1 lds\n\t"
        "buffer_load_dwordx4 %1, %2, 0 offen offset:3072 sc1 lds\n\t"
        "s_add_u32 m0, m0, 0x1000\n\ts_nop 0\n\t"
        "buffer_load_dwordx4 %1, %2, %4 offen sc1 lds\n\t"
        "buffer_load_dwordx4 %1, %2, %4 offen offset:1024 sc1 lds\n\t"
        "buffer_load_dwordx4 %1, %2, %4 offen offset:2048 sc1 lds\n\t"
        "buffer_load_dwordx4 %1, %2, %4 offen offset:3072 sc1 lds\n\t"
        "s_mov_b32 m0, %0"
        : "=&s"(keep) : "v"(voff), "s"(r), "s"(lds_dst), "s"(4096u) : "memory", "scc");
}

DI void poll4(__amdgpu_buffer_rsrc_t rws, unsigned off, unsigned epoch) {
#pragma unroll 1
    for (unsigned spin = 0; spin < SPIN_CAP; ++spin) {
        u32x4 v = __builtin_amdgcn_raw_buffer_load_b128(rws, off, 0, AUX_SC1);
        const unsigned d = (v[0] ^ epoch) | (v[1] ^ epoch) | (v[2] ^ epoch) | (v[3] ^ epoch);
        if (__all(d == 0)) break;
        __builtin_amdgcn_s_sleep(1);
    }
    __builtin_amdgcn_fence(__ATOMIC_ACQUIRE, "wavefront");
}

}

__global__ __launch_bounds__(512, 2) void mha_fused(const u16* __restrict__ x, const u16* __restrict__ kc,
                                                    const u16* __restrict__ vc, const u16* __restrict__ wq,
                                                    const u16* __restrict__ wo, u16* __restrict__ out,
                                                    char* __restrict__ ws) {
    __shared__ __attribute__((aligned(16))) char smem_[LDS_BYTES];
    LAS char* smem = (LAS char*)smem_;
    const int bid = blockIdx.x;
    const int g = bid & 7, j = (bid >> 3) & 3, b = bid >> 5;
    const int h = 4 * g + j;
    const int wave = __builtin_amdgcn_readfirstlane(threadIdx.x >> 6);
    const int lane = threadIdx.x & 63;
    const int hrot = (j + b) & 3;
    const int crot = b >> 2;
    STAMP_DECL

    if (__builtin_expect(wave >= 4, 1)) {
        const int lw = wave - 4;
        STAMP(0);
        TR_DECL
        TR();
        int ksA = ((b * 10) >> 3) + ((j * 5) >> 1); if (ksA >= 10) ksA -= 10;
        const __amdgpu_buffer_rsrc_t rx = __builtin_amdgcn_make_buffer_rsrc((void*)x, 0, NB * SQ * DM * 2, 0x00020000);
        const __amdgpu_buffer_rsrc_t rk = __builtin_amdgcn_make_buffer_rsrc((void*)kc, 0, NB * NH * SK * DH * 2, 0x00020000);
        const __amdgpu_buffer_rsrc_t rv = __builtin_amdgcn_make_buffer_rsrc((void*)vc, 0, NB * NH * SK * DH * 2, 0x00020000);
        const __amdgpu_buffer_rsrc_t rq = __builtin_amdgcn_make_buffer_rsrc((void*)wq, 0, DM * HD * 2, 0x00020000);
        const __amdgpu_buffer_rsrc_t ro = __builtin_amdgcn_make_buffer_rsrc((void*)wo, 0, HD * DM * 2, 0x00020000);
        const int r8 = lane >> 3, c8 = lane & 7;
        const unsigned sw8 = (unsigned)((c8 ^ r8) * 16);
        const unsigned swt = (unsigned)((2 * ((c8 >> 1) ^ ((r8 >> 1) & 1)) + (c8 & 1)) * 16);
        const unsigned vx = (unsigned)(r8 * (DM * 2)) + sw8;
        const unsigned vq0 = (unsigned)(r8 * (HD * 2)) + swt, vq1 = (unsigned)(r8 * (HD * 2)) + (swt ^ 64u);
        const unsigned vkv = (unsigned)(r8 * (DH * 2)) + sw8;
        const unsigned vo0 = (unsigned)(r8 * (DM * 2)) + swt, vo1 = (unsigned)(r8 * (DM * 2)) + (swt ^ 64u);
        const unsigned vh = (unsigned)((lane >> 2) * (DM * 2) + ((lane & 3) ^ ((lane >> 4) & 2)) * 16);
        const unsigned colF = (unsigned)(j * 320 + (j & 1) * 64), colH = (unsigned)(j * 320 + ((j & 1) ? 0 : 256));
#define DMA(rs, dst, voff, soff, aux) __builtin_amdgcn_raw_ptr_buffer_load_lds(rs, (LAS void*)(dst), 16, voff, soff, 0, aux)
        LAS char* const sw = smem + lw * 2048;
        const unsigned sx0 = (unsigned)((b * 64 + 16 * lw) * (DM * 2));
        const unsigned sq0 = (unsigned)(16 * lw * (HD * 2) + h * (DH * 2));
        auto issue_A = [&](int it, unsigned sx, unsigned so) {
            LAS char* sb = sw + (it & 3) * SLOT;
            DMA(rx, sb, vx, sx, AUX_X);
            DMA(rx, sb + 1024, vx, sx + 8 * DM * 2, AUX_X);
#pragma unroll
            for (int blk = 0; blk < 3; ++blk) {
                DMA(rq, sb + 8192 + blk * 8192, vq0, so + blk * 128, AUX_W);
                DMA(rq, sb + 8192 + blk * 8192 + 1024, vq1, so + blk * 128 + 8 * HD * 2, AUX_W);
            }
        };
#pragma unroll 1
        for (int it = 0; it < 10; ++it) {
            const unsigned sx = sx0 + ksA * 128, so = sq0 + ksA * 64 * (HD * 2);
            if (++ksA == 10) ksA = 0;
            SB();
            if (it == 2) { wait_vm<8>(); STAMP(1); BARL(); }
            else if (it >= 4) {
                wait_vm<16>();
                BARL();
            }
            SB();
            issue_A(it, sx, so);
        }
        {
            const unsigned sk = (unsigned)(((b * NH + h) * SK + 16 * lw) * (DH * 2));
            LAS char* sb = sw + (10 & 3) * SLOT;
            SB();
            wait_vm<16>(); BARL();
            SB();
#pragma unroll
            for (int st = 0; st < 3; ++st) {
                DMA(rk, sb + st * 8192, vkv, sk + st * 128, AUX_KV);
                DMA(rk, sb + st * 8192 + 1024, vkv, sk + st * 128 + 8 * DH * 2, AUX_KV);
            }
        }
        int ch = crot, hh = 4 * g + hrot;
#pragma unroll 1
        for (int it = 11; it < NITEM; ++it) {
            const unsigned sv = (unsigned)(((b * NH + hh) * SK + 16 * lw) * (DH * 2) + ch * 128);
            const unsigned so = (unsigned)((hh * DH + ch * 64 + 16 * lw) * (DM * 2));
            if (++ch == 3) ch = 0;
            if (ch == crot) hh = (hh & ~3) | ((hh + 1) & 3);
            LAS char* sb = sw + (it & 3) * SLOT;
            SB();
            if (it == 11) wait_vm<14>();
            else if (it == 12) wait_vm<13>();
            else if (it == 14) { wait_vm<21>(); BARL(); wait_vm<14>(); }
            else if (it > 14) wait_vm<14>();
            BARL();
            SB();
            DMA(rv, sb, vkv, sv, AUX_KV);
            DMA(rv, sb + 1024, vkv, sv + 8 * DH * 2, AUX_KV);
#pragma unroll
            for (int blk = 0; blk < 2; ++blk) {
                DMA(ro, sb + 8192 + blk * 8192, vo0, so + colF + blk * 128, AUX_W);
                DMA(ro, sb + 8192 + blk * 8192 + 1024, vo1, so + colF + blk * 128 + 8 * DM * 2, AUX_W);
            }
            DMA(ro, sb - lw * 1024 + 24576, vh, so + colH, AUX_W);
        }
        wait_vm<14>(); BARL();
        wait_vm<7>(); BARL();
        wait_vm<0>(); BARL();
        STAMP(2);
        TR_OUT();
        STAMP_OUT();
        return;
    }

    const int w = wave;
    STAMP(0);
    unsigned code_pf = 0;
    if (CODE_PF > 0) {
        unsigned long long pc;
        asm volatile("s_getpc_b64 %0" : "=s"(pc));
        const __amdgpu_buffer_rsrc_t rc = __builtin_amdgcn_make_buffer_rsrc((void*)(pc & ~127ull), 0, CODE_PF, 0x00020000);
        code_pf = __builtin_amdgcn_raw_buffer_load_b32(rc, (unsigned)((w * 64 + lane) * 128), 0, 0);
    }
    const int l15 = lane & 15, lg = lane >> 4;
    const __amdgpu_buffer_rsrc_t rws = __builtin_amdgcn_make_buffer_rsrc((void*)ws, 0, WS_END, 0x00020000);
    const unsigned epoch_v = __hip_atomic_load((gu32*)(ws + WS_E + b * 128), RLX_AGENT);

    const int an0 = l15 * 128 + ((lg ^ (l15 & 7)) << 4);
    const int q4 = l15 >> 2, p4 = l15 & 3;
    const int sq = ((q4 >> 1) & 1) | ((lg & 1) << 1);
    int aq[3], ao[3];
#pragma unroll
    for (int u = 0; u < 3; ++u) {
        const int T = 3 * w + u;
        aq[u] = 8192 + (T >> 2) * 8192 + (8 * lg + q4) * 128 + (((T & 3) ^ sq) << 5) + p4 * 8;
    }
#pragma unroll
    for (int u = 0; u < 2; ++u) {
        const int T = 2 * w + u;
        ao[u] = 8192 + (T >> 2) * 8192 + (8 * lg + q4) * 128 + (((T & 3) ^ sq) << 5) + p4 * 8;
    }
    ao[2] = 24576 + (8 * lg + q4) * 64 + (((w & 1) ^ (lg & 1)) << 5) + p4 * 8;

    auto load_f = [&](Frags& f, const int (&at)[3], LAS char* sb, int ks, int kst2, int hi2) {
        f.tr[0] = lds_tr(sb + at[0] + ks * 4096, 512);
#pragma unroll
        for (int t = 0; t < 4; ++t) f.nat[t] = lds_b128(sb + (an0 ^ (ks << 6)) + t * 2048);
        f.tr[1] = lds_tr(sb + at[1] + ks * 4096, 512);
        f.tr[2] = lds_tr(sb + at[2] + ks * kst2, hi2);
    };
    f32x4 acc[3][4];
#pragma unroll
    for (int u = 0; u < 3; ++u)
#pragma unroll
        for (int t = 0; t < 4; ++t) acc[u][t] = f32x4{0.f, 0.f, 0.f, 0.f};
    auto mma_A = [&](const Frags& f) {
#pragma unroll
        for (int u = 0; u < 3; ++u)
#pragma unroll
            for (int t = 0; t < 4; ++t) acc[u][t] = mfma(f.tr[u], f.nat[t], acc[u][t]);
    };
    auto mma_B = [&](const Frags& f) {
#pragma unroll
        for (int u = 0; u < 3; ++u)
#pragma unroll
            for (int t = 0; t < 4; ++t) acc[u][t] = mfma(f.nat[t], f.tr[u], acc[u][t]);
    };
    auto ilv_even = [&]() {
#pragma unroll
        for (int i = 0; i < 10; ++i) { SGB(0x100, 1); SGB(0x8, 1); }
        SGB(0x8, 2);
    };
    auto ilv_front = [&]() {
#pragma unroll
        for (int i = 0; i < 5; ++i) { SGB(0x100, 2); SGB(0x8, 1); }
        SGB(0x8, 7);
    };

    f32x4 oacc[3][4];
#pragma unroll
    for (int u = 0; u < 3; ++u)
#pragma unroll
        for (int t = 0; t < 4; ++t) { oacc[u][t] = f32x4{0.f, 0.f, 0.f, 0.f}; asm volatile("" : "+v"(oacc[u][t])); }
    Frags f0, f1;
#pragma unroll
    for (int t = 0; t < 4; ++t) f1.nat[t] = u32x4{0, 0, 0, 0};
#pragma unroll
    for (int u = 0; u < 3; ++u) f1.tr[u] = u32x4{0, 0, 0, 0};
#pragma unroll 1
    for (int t = 0; t < 10; ++t) {
        LAS char* sb = smem + (t & 3) * SLOT;
        bar();
        SB();
        load_f(f0, aq, sb, 0, 4096, 512);
        mma_A(f1);
        ilv_even();
        SB();
        load_f(f1, aq, sb, 1, 4096, 512);
        mma_A(f0);
        ilv_front();
        SB();
        wait_lgkm0();
    }
    STAMP(1);
    asm volatile("" :: "v"(code_pf));
    const unsigned epoch = (unsigned)__builtin_amdgcn_readfirstlane(epoch_v) + 1u;
    bar();
    mma_A(f1);
#pragma unroll
    for (int u = 0; u < 3; ++u) {
        const int T = 3 * w + u;
        LAS char* qb = smem + XREG + (T >> 2) * 8192 + l15 * 128 + (((2 * (T & 3) + (lg >> 1)) ^ (l15 & 7)) << 4) + (lg & 1) * 8;
#pragma unroll
        for (int t = 0; t < 4; ++t)
            *(LAS u32x2*)(qb + t * 2048) = u32x2{pk2(acc[u][t][0], acc[u][t][1]), pk2(acc[u][t][2], acc[u][t][3])};
    }
    wait_lgkm0();
    bar();
    {
        f32x4 s[4];
#pragma unroll
        for (int t = 0; t < 4; ++t) s[t] = f32x4{0.f, 0.f, 0.f, 0.f};
        LAS char* kb = smem + (10 & 3) * SLOT;
#pragma unroll 1
        for (int st = 0; st < 3; ++st) {
#pragma unroll
            for (int k2 = 0; k2 < 2; ++k2) {
                const int o = st * 8192 + (an0 ^ (k2 << 6));
                const u32x4 qf = lds_b128(smem + XREG + o + w * 2048);
#pragma unroll
                for (int t = 0; t < 4; ++t) s[t] = mfma(lds_b128(kb + o + t * 2048), qf, s[t]);
            }
        }
        float mx = fmaxf(fmaxf(s[0][0], s[0][1]), fmaxf(s[0][2], s[0][3]));
#pragma unroll
        for (int t = 1; t < 4; ++t) mx = fmaxf(fmaxf(fmaxf(mx, s[t][0]), fmaxf(s[t][1], s[t][2])), s[t][3]);
        mx = xmax(mx);
        constexpr float C = 0.07216878364870322f * 1.4426950408889634f;
        const float mc = mx * C;
        float sum = 0.f;
#pragma unroll
        for (int t = 0; t < 4; ++t)
#pragma unroll
            for (int i = 0; i < 4; ++i) { s[t][i] = __builtin_amdgcn_exp2f(fmaf(s[t][i], C, -mc)); sum += s[t][i]; }
        const float rs = __builtin_amdgcn_rcpf(xsum(sum));
#pragma unroll
        for (int s2 = 0; s2 < 2; ++s2) {
            const u32x4 pf = pack8(s[2 * s2] * rs, s[2 * s2 + 1] * rs);
            __builtin_amdgcn_raw_buffer_store_b128(pf, rws, WS_P + bid * 8192 + (w * 2 + s2) * 1024 + lane * 16, 0, AUX_SC1);
            *(LAS u32x4*)(smem + XREG + 24576 + (w * 2 + s2) * 1024 + lane * 16) = pf;
        }
    }
    wait_lgkm0();
    STAMP(2);

    u32x4 vwOld[3][2], vwMid[3][2];
#pragma unroll
    for (int u = 0; u < 3; ++u) asm volatile("" : "=v"(vwMid[u][0]), "=v"(vwMid[u][1]));
    auto cvt_vw = [&](u32x4 (&vw)[3][2]) {
#pragma unroll
        for (int u = 0; u < 3; ++u) {
            vw[u][0] = pack8(acc[u][0], acc[u][1]);
            vw[u][1] = pack8(acc[u][2], acc[u][3]);
#pragma unroll
            for (int t = 0; t < 4; ++t) acc[u][t] = f32x4{0.f, 0.f, 0.f, 0.f};
        }
    };
    auto do_C = [&](int m, int s2, const u32x4 (&vw)[3][2]) {
        const int sidx = ((m + hrot) - j + 3) & 3;
        LAS char* pb = smem + XREG + sidx * 8192 + lane * 16 + s2 * 1024;
        u32x4 pf[4];
#pragma unroll
        for (int t = 0; t < 4; ++t) pf[t] = lds_b128(pb + t * 2048);
#pragma unroll
        for (int t = 0; t < 4; ++t)
#pragma unroll
            for (int u = 0; u < 3; ++u) oacc[u][t] = mfma(vw[u][s2], pf[t], oacc[u][t]);
    };
#pragma unroll 1
    for (int m = 0; m < 4; ++m) {
        const int slot0 = (11 + 3 * m) & 3;
        LAS char* sb = smem + slot0 * SLOT;
        bar();
        SB();
        load_f(f0, ao, sb, 0, 2048, 256);
        mma_B(f1);
        ilv_even();
        SB();
#pragma unroll
        for (int u = 0; u < 3; ++u) { vwOld[u][0] = vwMid[u][0]; vwOld[u][1] = vwMid[u][1]; }
        cvt_vw(vwMid);
        SB();
        load_f(f1, ao, sb, 1, 2048, 256);
        mma_B(f0);
        ilv_front();
        SB();
        if (m == 2) wait_vm<0>();
        if (m == 3) do_C(1, 0, vwOld);
        wait_lgkm0();
        sb = smem + ((slot0 + 1) & 3) * SLOT;
        bar();
        SB();
        load_f(f0, ao, sb, 0, 2048, 256);
        mma_B(f1);
        ilv_even();
        SB();
        load_f(f1, ao, sb, 1, 2048, 256);
        mma_B(f0);
        ilv_front();
        SB();
        if (m == 0) {
            wait_vm<0>();
            if (lane == 0) __hip_atomic_store((gu32*)(ws + WS_PF + bid * 128 + w * 4), epoch, RLX_AGENT);
        }
        if (m == 1 && w > 0) {
            const int src = (bid & ~24) | (((j + w) & 3) << 3);
            poll4(rws, WS_PF + src * 128, epoch);
            dma_p(rws, WS_P + src * 8192 + lane * 16, (unsigned)(XREG + (w - 1) * 8192));
        }
        if (m == 2) do_C(0, 0, vwOld);
        if (m == 3) { do_C(1, 1, vwOld); do_C(2, 0, vwMid); }
        wait_lgkm0();
        sb = smem + ((slot0 + 2) & 3) * SLOT;
        bar();
        SB();
        load_f(f0, ao, sb, 0, 2048, 256);
        mma_B(f1);
        ilv_even();
        SB();
        load_f(f1, ao, sb, 1, 2048, 256);
        mma_B(f0);
        ilv_front();
        SB();
        if (m == 2) do_C(0, 1, vwOld);
        if (m == 3) do_C(2, 1, vwMid);
        wait_lgkm0();
    }
    STAMP(3);
    mma_B(f1);
    cvt_vw(vwMid);
    do_C(3, 0, vwMid);
    do_C(3, 1, vwMid);
    STAMP(4);

    {
        const unsigned ob = WS_IN + (((bid & ~7) | (l15 >> 2)) * 8 + g) * 2560 + (lg * 4 + (l15 & 3)) * 16;
#pragma unroll
        for (int u = 0; u < 3; ++u) {
            if (u < 2 || w < 2) {
#pragma unroll
                for (int m = 0; m < 2; ++m)
                    __builtin_amdgcn_raw_buffer_store_b128(pack8(oacc[u][2 * m], oacc[u][2 * m + 1]), rws,
                                                           ob + m * (4 * 8 * 2560) + (u < 2 ? 2 * w + u : 8 + w) * 256, 0, AUX_SC1);
            }
        }
        STAMP(5);
        wait_vm<0>();
        STAMP(6);
        if (lane == 0) __hip_atomic_store((gu32*)(ws + WS_OF + bid * 128 + w * 4), epoch, RLX_AGENT);
        if (w == 0) poll4(rws, WS_OF + ((bid & ~7) | (lane & 7)) * 128, epoch);
        STAMP(7);
        bar();
        const int T = w * 64 + lane;
        if (T < 160) {
            u32x4 v[8];
#pragma unroll
            for (int s = 0; s < 8; ++s)
                v[s] = __builtin_amdgcn_raw_buffer_load_b128(rws, WS_IN + (bid * 8 + s) * 2560 + T * 16, 0, AUX_SC1);
            float r[8];
#pragma unroll
            for (int i = 0; i < 4; ++i) { r[2 * i] = bflo(v[0][i]); r[2 * i + 1] = bfhi(v[0][i]); }
#pragma unroll
            for (int s = 1; s < 8; ++s)
#pragma unroll
                for (int i = 0; i < 4; ++i) { r[2 * i] += bflo(v[s][i]); r[2 * i + 1] += bfhi(v[s][i]); }
            const int U = T >> 4, gg = (T >> 2) & 3, qq = T & 3;
            const int row = 32 * (g >> 2) + 4 * (g & 3) + qq;
            const int col = (j & 1) ? (U < 8 ? 32 + 16 * U : 16 * (U - 8)) : 16 * U;
            u16* o = out + (b * SQ + row) * DM + j * 160 + col + 4 * gg;
            __hip_atomic_store((GAS unsigned long long*)o, ((unsigned long long)pk2(r[2], r[3]) << 32) | pk2(r[0], r[1]), RLX_AGENT);
            __hip_atomic_store((GAS unsigned long long*)(o + 16 * DM), ((unsigned long long)pk2(r[6], r[7]) << 32) | pk2(r[4], r[5]), RLX_AGENT);
        }
        STAMP(8);
        STAMP(9);
        STAMP_OUT();
        if (g == 0 && j == 0 && threadIdx.x == 0) __hip_atomic_store((gu32*)(ws + WS_E + b * 128), epoch, RLX_AGENT);
    }
}

extern "C" void kernel_launch(void* const* d_in, const int* in_sizes, int n_in, void* d_out, int out_size,
                              void* d_ws, size_t ws_size, hipStream_t stream) {
    mha_fused<<<256, 512, 0, stream>>>((const u16*)d_in[0], (const u16*)d_in[1], (const u16*)d_in[2],
                                       (const u16*)d_in[3], (const u16*)d_in[4], (u16*)d_out, (char*)d_ws);
}
```

```cpp
#include <hip/hip_runtime.h>
#include <stdint.h>

#define LAS __attribute__((address_space(3)))
#define GAS __attribute__((address_space(1)))
typedef unsigned short u16;
typedef short s16x4 __attribute__((ext_vector_type(4)));
typedef short s16x8 __attribute__((ext_vector_type(8)));
typedef float f32x4 __attribute__((ext_vector_type(4)));
typedef float f32x2 __attribute__((ext_vector_type(2)));
typedef __bf16 bf16x2 __attribute__((ext_vector_type(2)));
typedef unsigned u32x4 __attribute__((ext_vector_type(4)));
typedef unsigned u32x2 __attribute__((ext_vector_type(2)));
typedef GAS unsigned gu32;
#define RLX_AGENT __ATOMIC_RELAXED, __HIP_MEMORY_SCOPE_AGENT
#define DI __device__ __forceinline__

namespace {

constexpr int NB = 8, SQ = 64, DM = 640, NH = 32, SK = 64, DH = 192, HD = NH * DH;
constexpr int AUX_SC1 = 16;
#define AUX_W 0
#define AUX_KV 0
#define AUX_X 0
#define CODE_PF 7168

constexpr int SLOT = 32768;
constexpr int XREG = 4 * SLOT;
constexpr int LDS_BYTES = 163840;

constexpr unsigned WS_E = 0;
constexpr unsigned WS_PF = 4096;
constexpr unsigned WS_OF = WS_PF + 256 * 128;
constexpr unsigned WS_P = 131072;
constexpr unsigned WS_IN = WS_P + 256 * 8192;
constexpr unsigned WS_END = WS_IN + 256 * 8 * 2560;
constexpr unsigned SPIN_CAP = 1u << 18;
constexpr unsigned VOOB = 0x80000000u;
constexpr int NITEM = 23;

#define STAMP(i)
#define STAMP_DECL
#define STAMP_OUT()
#define TR_DECL
#define TR()
#define TR_OUT()

template <int N> DI void wait_vm() { asm volatile("s_waitcnt vmcnt(%0)" ::"i"(N) : "memory"); }
DI void bar() { asm volatile("s_barrier" ::: "memory"); }
#define BARL() do { bar(); TR(); } while (0)
#define SB() __builtin_amdgcn_sched_barrier(0)
#define SGB(mask, n) __builtin_amdgcn_sched_group_barrier(mask, n, 0)
DI void wait_lgkm0() { __builtin_amdgcn_s_waitcnt(0xC07F); asm volatile("" ::: "memory"); }

DI f32x4 mfma(u32x4 a, u32x4 b, f32x4 c) {
    return __builtin_amdgcn_mfma_f32_16x16x32_bf16(__builtin_bit_cast(s16x8, a), __builtin_bit_cast(s16x8, b), c, 0, 0, 0);
}
DI u32x4 lds_b128(LAS char* p) { return *(LAS u32x4*)p; }
DI u32x4 lds_tr(LAS char* p, int hi_off) {
    u32x2 lo = __builtin_bit_cast(u32x2, __builtin_amdgcn_ds_read_tr16_b64_v4i16((LAS s16x4*)p));
    u32x2 hi = __builtin_bit_cast(u32x2, __builtin_amdgcn_ds_read_tr16_b64_v4i16((LAS s16x4*)(p + hi_off)));
    return u32x4{lo[0], lo[1], hi[0], hi[1]};
}
DI unsigned pk2(float a, float b) {
    f32x2 v = {a, b};
    return __builtin_bit_cast(unsigned, __builtin_convertvector(v, bf16x2));
}
DI u32x4 pack8(f32x4 a, f32x4 b) { return u32x4{pk2(a[0], a[1]), pk2(a[2], a[3]), pk2(b[0], b[1]), pk2(b[2], b[3])}; }
DI float bflo(unsigned w) { return __uint_as_float(w << 16); }
DI float bfhi(unsigned w) { return __uint_as_float(w & 0xffff0000u); }
DI float xmax(float v) {
    auto a = __builtin_amdgcn_permlane16_swap(__float_as_uint(v), __float_as_uint(v), false, false);
    v = fmaxf(__uint_as_float(a[0]), __uint_as_float(a[1]));
    auto c = __builtin_amdgcn_permlane32_swap(__float_as_uint(v), __float_as_uint(v), false, false);
    return fmaxf(__uint_as_float(c[0]), __uint_as_float(c[1]));
}
DI float xsum(float v) {
    auto a = __builtin_amdgcn_permlane16_swap(__float_as_uint(v), __float_as_uint(v), false, false);
    v = __uint_as_float(a[0]) + __uint_as_float(a[1]);
    auto c = __builtin_amdgcn_permlane32_swap(__float_as_uint(v), __float_as_uint(v), false, false);
    return __uint_as_float(c[0]) + __uint_as_float(c[1]);
}

struct Frags { u32x4 nat[4]; u32x4 tr[3]; };

DI void dma_p(__amdgpu_buffer_rsrc_t r, unsigned voff, unsigned lds_dst) {
    unsigned keep;
    asm volatile(
        "s_nop 4\n\ts_mov_b32 %0, m0\n\t"
        "s_mov_b32 m0, %3\n\ts_nop 0\n\t"
        "buffer_load_dwordx4 %1, %2, 0 offen sc1 lds\n\t"
        "buffer_load_dwordx4 %1, %2, 0 offen offset:1024 sc1 lds\n\t"
        "buffer_load_dwordx4 %1, %2, 0 offen offset:2048 sc1 lds\n\t"
        "buffer_load_dwordx4 %1, %2, 0 offen offset:3072 sc1 lds\n\t"
        "s_add_u32 m0, m0, 0x1000\n\ts_nop 0\n\t"
        "buffer_load_dwordx4 %1, %2, %4 offen sc1 lds\n\t"
        "buffer_load_dwordx4 %1, %2, %4 offen offset:1024 sc1 lds\n\t"
        "buffer_load_dwordx4 %1, %2, %4 offen offset:2048 sc1 lds\n\t"
        "buffer_load_dwordx4 %1, %2, %4 offen offset:3072 sc1 lds\n\t"
        "s_mov_b32 m0, %0"
        : "=&s"(keep) : "v"(voff), "s"(r), "s"(lds_dst), "s"(4096u) : "memory", "scc");
}

DI void poll4(__amdgpu_buffer_rsrc_t rws, unsigned off, unsigned epoch) {
#pragma unroll 1
    for (unsigned spin = 0; spin < SPIN_CAP; ++spin) {
        u32x4 v = __builtin_amdgcn_raw_buffer_load_b128(rws, off, 0, AUX_SC1);
        const unsigned d = (v[0] ^ epoch) | (v[1] ^ epoch) | (v[2] ^ epoch) | (v[3] ^ epoch);
        if (__all(d == 0)) break;
        __builtin_amdgcn_s_sleep(1);
    }
    __builtin_amdgcn_fence(__ATOMIC_ACQUIRE, "wavefront");
}

}

__global__ __launch_bounds__(512, 2) void mha_fused(const u16* __restrict__ x, const u16* __restrict__ kc,
                                                    const u16* __restrict__ vc, const u16* __restrict__ wq,
                                                    const u16* __restrict__ wo, u16* __restrict__ out,
                                                    char* __restrict__ ws) {
    __shared__ __attribute__((aligned(16))) char smem_[LDS_BYTES];
    LAS char* smem = (LAS char*)smem_;
    const int bid = blockIdx.x;
    const int g = bid & 7, j = (bid >> 3) & 3, b = bid >> 5;
    const int h = 4 * g + j;
    const int wave = __builtin_amdgcn_readfirstlane(threadIdx.x >> 6);
    const int lane = threadIdx.x & 63;
    const int hrot = (j + b) & 3;
    const int crot = b >> 2;
    STAMP_DECL

    if (__builtin_expect(wave >= 4, 1)) {
        const int lw = wave - 4;
        STAMP(0);
        TR_DECL
        TR();
        int ksA = ((b * 10) >> 3) + ((j * 5) >> 1); if (ksA >= 10) ksA -= 10;
        const __amdgpu_buffer_rsrc_t rx = __builtin_amdgcn_make_buffer_rsrc((void*)x, 0, NB * SQ * DM * 2, 0x00020000);
        const __amdgpu_buffer_rsrc_t rk = __builtin_amdgcn_make_buffer_rsrc((void*)kc, 0, NB * NH * SK * DH * 2, 0x00020000);
        const __amdgpu_buffer_rsrc_t rv = __builtin_amdgcn_make_buffer_rsrc((void*)vc, 0, NB * NH * SK * DH * 2, 0x00020000);
        const __amdgpu_buffer_rsrc_t rq = __builtin_amdgcn_make_buffer_rsrc((void*)wq, 0, DM * HD * 2, 0x00020000);
        const __amdgpu_buffer_rsrc_t ro = __builtin_amdgcn_make_buffer_rsrc((void*)wo, 0, HD * DM * 2, 0x00020000);
        const int r8 = lane >> 3, c8 = lane & 7;
        const unsigned sw8 = (unsigned)((c8 ^ r8) * 16);
        const unsigned swt = (unsigned)((2 * ((c8 >> 1) ^ ((r8 >> 1) & 1)) + (c8 & 1)) * 16);
        const unsigned vx = (unsigned)(r8 * (DM * 2)) + sw8;
        const unsigned vq0 = (unsigned)(r8 * (HD * 2)) + swt, vq1 = (unsigned)(r8 * (HD * 2)) + (swt ^ 64u);
        const unsigned vkv = (unsigned)(r8 * (DH * 2)) + sw8;
        const unsigned vo0 = (unsigned)(r8 * (DM * 2)) + swt, vo1 = (unsigned)(r8 * (DM * 2)) + (swt ^ 64u);
        const unsigned vh = (unsigned)((lane >> 2) * (DM * 2) + ((lane & 3) ^ ((lane >> 4) & 2)) * 16);
        const unsigned colF = (unsigned)(j * 320 + (j & 1) * 64), colH = (unsigned)(j * 320 + ((j & 1) ? 0 : 256));
#define DMA(rs, dst, voff, soff, aux) __builtin_amdgcn_raw_ptr_buffer_load_lds(rs, (LAS void*)(dst), 16, voff, soff, 0, aux)
        LAS char* const sw = smem + lw * 2048;
        const unsigned sx0 = (unsigned)((b * 64 + 16 * lw) * (DM * 2));
        const unsigned sq0 = (unsigned)(16 * lw * (HD * 2) + h * (DH * 2));
        auto issue_A = [&](int it, unsigned sx, unsigned so) {
            LAS char* sb = sw + (it & 3) * SLOT;
            DMA(rx, sb, vx, sx, AUX_X);
            DMA(rx, sb + 1024, vx, sx + 8 * DM * 2, AUX_X);
#pragma unroll
            for (int blk = 0; blk < 3; ++blk) {
                DMA(rq, sb + 8192 + blk * 8192, vq0, so + blk * 128, AUX_W);
                DMA(rq, sb + 8192 + blk * 8192 + 1024, vq1, so + blk * 128 + 8 * HD * 2, AUX_W);
            }
        };
#pragma unroll 1
        for (int it = 0; it < 10; ++it) {
            const unsigned sx = sx0 + ksA * 128, so = sq0 + ksA * 64 * (HD * 2);
            if (++ksA == 10) ksA = 0;
            SB();
            if (it == 2) { wait_vm<8>(); STAMP(1); BARL(); }
            else if (it >= 4) {
                wait_vm<16>();
                BARL();
            }
            SB();
            issue_A(it, sx, so);
        }
        {
            const unsigned sk = (unsigned)(((b * NH + h) * SK + 16 * lw) * (DH * 2));
            LAS char* sb = sw + (10 & 3) * SLOT;
            SB();
            wait_vm<16>(); BARL();
            SB();
#pragma unroll
            for (int st = 0; st < 3; ++st) {
                DMA(rk, sb + st * 8192, vkv, sk + st * 128, AUX_KV);
                DMA(rk, sb + st * 8192 + 1024, vkv, sk + st * 128 + 8 * DH * 2, AUX_KV);
            }
        }
        int ch = crot, hh = 4 * g + hrot;
#pragma unroll 1
        for (int it = 11; it < NITEM; ++it) {
            const unsigned sv = (unsigned)(((b * NH + hh) * SK + 16 * lw) * (DH * 2) + ch * 128);
            const unsigned so = (unsigned)((hh * DH + ch * 64 + 16 * lw) * (DM * 2));
            if (++ch == 3) ch = 0;
            if (ch == crot) hh = (hh & ~3) | ((hh + 1) & 3);
            LAS char* sb = sw + (it & 3) * SLOT;
            SB();
            if (it == 11) wait_vm<14>();
            else if (it == 12) wait_vm<13>();
            else if (it == 14) { wait_vm<21>(); BARL(); wait_vm<14>(); }
            else if (it > 14) wait_vm<14>();
            BARL();
            SB();
            DMA(rv, sb, vkv, sv, AUX_KV);
            DMA(rv, sb + 1024, vkv, sv + 8 * DH * 2, AUX_KV);
#pragma unroll
            for (int blk = 0; blk < 2; ++blk) {
                DMA(ro, sb + 8192 + blk * 8192, vo0, so + colF + blk * 128, AUX_W);
                DMA(ro, sb + 8192 + blk * 8192 + 1024, vo1, so + colF + blk * 128 + 8 * DM * 2, AUX_W);
            }
            DMA(ro, sb - lw * 1024 + 24576, vh, so + colH, AUX_W);
        }
        wait_vm<14>(); BARL();
        wait_vm<7>(); BARL();
        wait_vm<0>(); BARL();
        STAMP(2);
        TR_OUT();
        STAMP_OUT();
        return;
    }

    const int w = wave;
    STAMP(0);
    unsigned code_pf = 0;
    if (CODE_PF > 0) {
        unsigned long long pc;
        asm volatile("s_getpc_b64 %0" : "=s"(pc));
        const __amdgpu_buffer_rsrc_t rc = __builtin_amdgcn_make_buffer_rsrc((void*)(pc & ~127ull), 0, CODE_PF, 0x00020000);
        code_pf = __builtin_amdgcn_raw_buffer_load_b32(rc, (unsigned)((w * 64 + lane) * 128), 0, 0);
    }
    const int l15 = lane & 15, lg = lane >> 4;
    const __amdgpu_buffer_rsrc_t rws = __builtin_amdgcn_make_buffer_rsrc((void*)ws, 0, WS_END, 0x00020000);
    const unsigned epoch_v = __hip_atomic_load((gu32*)(ws + WS_E + b * 128), RLX_AGENT);

    const int an0 = l15 * 128 + ((lg ^ (l15 & 7)) << 4);
    const int q4 = l15 >> 2, p4 = l15 & 3;
    const int sq = ((q4 >> 1) & 1) | ((lg & 1) << 1);
    int aq[3], ao[3];
#pragma unroll
    for (int u = 0; u < 3; ++u) {
        const int T = 3 * w + u;
        aq[u] = 8192 + (T >> 2) * 8192 + (8 * lg + q4) * 128 + (((T & 3) ^ sq) << 5) + p4 * 8;
    }
#pragma unroll
    for (int u = 0; u < 2; ++u) {
        const int T = 2 * w + u;
        ao[u] = 8192 + (T >> 2) * 8192 + (8 * lg + q4) * 128 + (((T & 3) ^ sq) << 5) + p4 * 8;
    }
    ao[2] = 24576 + (8 * lg + q4) * 64 + (((w & 1) ^ (lg & 1)) << 5) + p4 * 8;

    auto load_f = [&](Frags& f, const int (&at)[3], LAS char* sb, int ks, int kst2, int hi2) {
        f.tr[0] = lds_tr(sb + at[0] + ks * 4096, 512);
#pragma unroll
        for (int t = 0; t < 4; ++t) f.nat[t] = lds_b128(sb + (an0 ^ (ks << 6)) + t * 2048);
        f.tr[1] = lds_tr(sb + at[1] + ks * 4096, 512);
        f.tr[2] = lds_tr(sb + at[2] + ks * kst2, hi2);
    };
    f32x4 acc[3][4];
#pragma unroll
    for (int u = 0; u < 3; ++u)
#pragma unroll
        for (int t = 0; t < 4; ++t) acc[u][t] = f32x4{0.f, 0.f, 0.f, 0.f};
    auto mma_A = [&](const Frags& f) {
#pragma unroll
        for (int u = 0; u < 3; ++u)
#pragma unroll
            for (int t = 0; t < 4; ++t) acc[u][t] = mfma(f.tr[u], f.nat[t], acc[u][t]);
    };
    auto mma_B = [&](const Frags& f) {
#pragma unroll
        for (int u = 0; u < 3; ++u)
#pragma unroll
            for (int t = 0; t < 4; ++t) acc[u][t] = mfma(f.nat[t], f.tr[u], acc[u][t]);
    };
    auto ilv_even = [&]() {
#pragma unroll
        for (int i = 0; i < 10; ++i) { SGB(0x100, 1); SGB(0x8, 1); }
        SGB(0x8, 2);
    };
    auto ilv_front = [&]() {
#pragma unroll
        for (int i = 0; i < 5; ++i) { SGB(0x100, 2); SGB(0x8, 1); }
        SGB(0x8, 7);
    };

    f32x4 oacc[3][4];
#pragma unroll
    for (int u = 0; u < 3; ++u)
#pragma unroll
        for (int t = 0; t < 4; ++t) { oacc[u][t] = f32x4{0.f, 0.f, 0.f, 0.f}; asm volatile("" : "+v"(oacc[u][t])); }
    Frags f0, f1;
#pragma unroll
    for (int t = 0; t < 4; ++t) f1.nat[t] = u32x4{0, 0, 0, 0};
#pragma unroll
    for (int u = 0; u < 3; ++u) f1.tr[u] = u32x4{0, 0, 0, 0};
#pragma unroll 1
    for (int t = 0; t < 10; ++t) {
        LAS char* sb = smem + (t & 3) * SLOT;
        bar();
        SB();
        load_f(f0, aq, sb, 0, 4096, 512);
        mma_A(f1);
        ilv_even();
        SB();
        load_f(f1, aq, sb, 1, 4096, 512);
        mma_A(f0);
        ilv_front();
        SB();
        wait_lgkm0();
    }
    STAMP(1);
    asm volatile("" :: "v"(code_pf));
    const unsigned epoch = (unsigned)__builtin_amdgcn_readfirstlane(epoch_v) + 1u;
    bar();
    mma_A(f1);
#pragma unroll
    for (int u = 0; u < 3; ++u) {
        const int T = 3 * w + u;
        LAS char* qb = smem + XREG + (T >> 2) * 8192 + l15 * 128 + (((2 * (T & 3) + (lg >> 1)) ^ (l15 & 7)) << 4) + (lg & 1) * 8;
#pragma unroll
        for (int t = 0; t < 4; ++t)
            *(LAS u32x2*)(qb + t * 2048) = u32x2{pk2(acc[u][t][0], acc[u][t][1]), pk2(acc[u][t][2], acc[u][t][3])};
    }
    wait_lgkm0();
    bar();
    {
        f32x4 s[4];
#pragma unroll
        for (int t = 0; t < 4; ++t) s[t] = f32x4{0.f, 0.f, 0.f, 0.f};
        LAS char* kb = smem + (10 & 3) * SLOT;
#pragma unroll 1
        for (int st = 0; st < 3; ++st) {
#pragma unroll
            for (int k2 = 0; k2 < 2; ++k2) {
                const int o = st * 8192 + (an0 ^ (k2 << 6));
                const u32x4 qf = lds_b128(smem + XREG + o + w * 2048);
#pragma unroll
                for (int t = 0; t < 4; ++t) s[t] = mfma(lds_b128(kb + o + t * 2048), qf, s[t]);
            }
        }
        float mx = fmaxf(fmaxf(s[0][0], s[0][1]), fmaxf(s[0][2], s[0][3]));
#pragma unroll
        for (int t = 1; t < 4; ++t) mx = fmaxf(fmaxf(fmaxf(mx, s[t][0]), fmaxf(s[t][1], s[t][2])), s[t][3]);
        mx = xmax(mx);
        constexpr float C = 0.07216878364870322f * 1.4426950408889634f;
        const float mc = mx * C;
        float sum = 0.f;
#pragma unroll
        for (int t = 0; t < 4; ++t)
#pragma unroll
            for (int i = 0; i < 4; ++i) { s[t][i] = __builtin_amdgcn_exp2f(fmaf(s[t][i], C, -mc)); sum += s[t][i]; }
        const float rs = __builtin_amdgcn_rcpf(xsum(sum));
#pragma unroll
        for (int s2 = 0; s2 < 2; ++s2) {
            const u32x4 pf = pack8(s[2 * s2] * rs, s[2 * s2 + 1] * rs);
            __builtin_amdgcn_raw_buffer_store_b128(pf, rws, WS_P + bid * 8192 + (w * 2 + s2) * 1024 + lane * 16, 0, AUX_SC1);
            *(LAS u32x4*)(smem + XREG + 24576 + (w * 2 + s2) * 1024 + lane * 16) = pf;
        }
    }
    wait_lgkm0();
    STAMP(2);

    u32x4 vwOld[3][2], vwMid[3][2];
#pragma unroll
    for (int u = 0; u < 3; ++u) asm volatile("" : "=v"(vwMid[u][0]), "=v"(vwMid[u][1]));
    auto cvt_vw = [&](u32x4 (&vw)[3][2]) {
#pragma unroll
        for (int u = 0; u < 3; ++u) {
            vw[u][0] = pack8(acc[u][0], acc[u][1]);
            vw[u][1] = pack8(acc[u][2], acc[u][3]);
#pragma unroll
            for (int t = 0; t < 4; ++t) acc[u][t] = f32x4{0.f, 0.f, 0.f, 0.f};
        }
    };
    auto do_C = [&](int m, int s2, const u32x4 (&vw)[3][2]) {
        const int sidx = ((m + hrot) - j + 3) & 3;
        LAS char* pb = smem + XREG + sidx * 8192 + lane * 16 + s2 * 1024;
        u32x4 pf[4];
#pragma unroll
        for (int t = 0; t < 4; ++t) pf[t] = lds_b128(pb + t * 2048);
#pragma unroll
        for (int t = 0; t < 4; ++t)
#pragma unroll
            for (int u = 0; u < 3; ++u) oacc[u][t] = mfma(vw[u][s2], pf[t], oacc[u][t]);
    };
#pragma unroll 1
    for (int m = 0; m < 4; ++m) {
        const int slot0 = (11 + 3 * m) & 3;
        LAS char* sb = smem + slot0 * SLOT;
        bar();
        SB();
        load_f(f0, ao, sb, 0, 2048, 256);
        mma_B(f1);
        ilv_even();
        SB();
#pragma unroll
        for (int u = 0; u < 3; ++u) { vwOld[u][0] = vwMid[u][0]; vwOld[u][1] = vwMid[u][1]; }
        cvt_vw(vwMid);
        SB();
        load_f(f1, ao, sb, 1, 2048, 256);
        mma_B(f0);
        ilv_front();
        SB();
        if (m == 2) wait_vm<0>();
        if (m == 3) do_C(1, 0, vwOld);
        wait_lgkm0();
        sb = smem + ((slot0 + 1) & 3) * SLOT;
        bar();
        SB();
        load_f(f0, ao, sb, 0, 2048, 256);
        mma_B(f1);
        ilv_even();
        SB();
        load_f(f1, ao, sb, 1, 2048, 256);
        mma_B(f0);
        ilv_front();
        SB();
        if (m == 0) {
            wait_vm<0>();
            if (lane == 0) __hip_atomic_store((gu32*)(ws + WS_PF + bid * 128 + w * 4), epoch, RLX_AGENT);
        }
        if (m == 1 && w > 0) {
            const int src = (bid & ~24) | (((j + w) & 3) << 3);
            poll4(rws, WS_PF + src * 128, epoch);
            dma_p(rws, WS_P + src * 8192 + lane * 16, (unsigned)(XREG + (w - 1) * 8192));
        }
        if (m == 2) do_C(0, 0, vwOld);
        if (m == 3) { do_C(1, 1, vwOld); do_C(2, 0, vwMid); }
        wait_lgkm0();
        sb = smem + ((slot0 + 2) & 3) * SLOT;
        bar();
        SB();
        load_f(f0, ao, sb, 0, 2048, 256);
        mma_B(f1);
        ilv_even();
        SB();
        load_f(f1, ao, sb, 1, 2048, 256);
        mma_B(f0);
        ilv_front();
        SB();
        if (m == 2) do_C(0, 1, vwOld);
        if (m == 3) do_C(2, 1, vwMid);
        wait_lgkm0();
    }
    STAMP(3);
    mma_B(f1);
    cvt_vw(vwMid);
    do_C(3, 0, vwMid);
    do_C(3, 1, vwMid);
    STAMP(4);

    {
        const unsigned ob = WS_IN + (((bid & ~7) | (l15 >> 2)) * 8 + g) * 2560 + (lg * 4 + (l15 & 3)) * 16;
#pragma unroll
        for (int u = 0; u < 3; ++u) {
            if (u < 2 || w < 2) {
#pragma unroll
                for (int m = 0; m < 2; ++m)
                    __builtin_amdgcn_raw_buffer_store_b128(pack8(oacc[u][2 * m], oacc[u][2 * m + 1]), rws,
                                                           ob + m * (4 * 8 * 2560) + (u < 2 ? 2 * w + u : 8 + w) * 256, 0, AUX_SC1);
            }
        }
        STAMP(5);
        wait_vm<0>();
        STAMP(6);
        if (lane == 0) __hip_atomic_store((gu32*)(ws + WS_OF + bid * 128 + w * 4), epoch, RLX_AGENT);
        if (w == 0) poll4(rws, WS_OF + ((bid & ~7) | (lane & 7)) * 128, epoch);
        STAMP(7);
        bar();
        const int T = w * 64 + lane;
        if (T < 160) {
            u32x4 v[8];
#pragma unroll
            for (int s = 0; s < 8; ++s)
                v[s] = __builtin_amdgcn_raw_buffer_load_b128(rws, WS_IN + (bid * 8 + s) * 2560 + T * 16, 0, AUX_SC1);
            float r[8];
#pragma unroll
            for (int i = 0; i < 4; ++i) { r[2 * i] = bflo(v[0][i]); r[2 * i + 1] = bfhi(v[0][i]); }
#pragma unroll
            for (int s = 1; s < 8; ++s)
#pragma unroll
                for (int i = 0; i < 4; ++i) { r[2 * i] += bflo(v[s][i]); r[2 * i + 1] += bfhi(v[s][i]); }
            const int U = T >> 4, gg = (T >> 2) & 3, qq = T & 3;
            const int row = 32 * (g >> 2) + 4 * (g & 3) + qq;
            const int col = (j & 1) ? (U < 8 ? 32 + 16 * U : 16 * (U - 8)) : 16 * U;
            u16* o = out + (b * SQ + row) * DM + j * 160 + col + 4 * gg;
            *(u32x2*)o = u32x2{pk2(r[0], r[1]), pk2(r[2], r[3])};
            *(u32x2*)(o + 16 * DM) = u32x2{pk2(r[4], r[5]), pk2(r[6], r[7])};
        }
        STAMP(8);
        STAMP(9);
        STAMP_OUT();
        if (g == 0 && j == 0 && threadIdx.x == 0) __hip_atomic_store((gu32*)(ws + WS_E + b * 128), epoch, RLX_AGENT);
    }
}

__global__ void tiny1(char* ws) { if (ws == nullptr) __builtin_trap(); }
extern "C" void kernel_launch(void* const* d_in, const int* in_sizes, int n_in, void* d_out, int out_size,
                              void* d_ws, size_t ws_size, hipStream_t stream) {
    tiny1<<<1, 64, 0, stream>>>((char*)d_ws);
    mha_fused<<<256, 512, 0, stream>>>((const u16*)d_in[0], (const u16*)d_in[1], (const u16*)d_in[2],
                                       (const u16*)d_in[3], (const u16*)d_in[4], (u16*)d_out, (char*)d_ws);
}
```

```cpp
#include <hip/hip_runtime.h>
#include <stdint.h>
#include <type_traits>

#define LAS __attribute__((address_space(3)))
#define GAS __attribute__((address_space(1)))
typedef unsigned short u16;
typedef short s16x4 __attribute__((ext_vector_type(4)));
typedef short s16x8 __attribute__((ext_vector_type(8)));
typedef float f32x4 __attribute__((ext_vector_type(4)));
typedef float f32x2 __attribute__((ext_vector_type(2)));
typedef __bf16 bf16x2 __attribute__((ext_vector_type(2)));
typedef unsigned u32x4 __attribute__((ext_vector_type(4)));
typedef unsigned u32x2 __attribute__((ext_vector_type(2)));
typedef GAS unsigned gu32;
#define RLX_AGENT __ATOMIC_RELAXED, __HIP_MEMORY_SCOPE_AGENT
#define DI __device__ __forceinline__

namespace {

constexpr int NB = 8, SQ = 64, DM = 640, NH = 32, SK = 64, DH = 192, HD = NH * DH;
constexpr int AUX_SC1 = 16;

constexpr int SLOT = 32768;
constexpr int L_K = 3 * SLOT;
constexpr int L_Q = L_K + 24576;
constexpr int L_POWN = L_Q + 24576;
constexpr int LDS_BYTES = 163840;

constexpr unsigned WS_E = 0;
constexpr unsigned WS_PF = 4096;
constexpr unsigned WS_OF = WS_PF + 256 * 128;
constexpr unsigned WS_P = 131072;
constexpr unsigned INB = 3072;
constexpr unsigned WS_IN = WS_P + 256 * 8192;
constexpr unsigned WS_END = WS_IN + 256 * 8 * INB;
constexpr unsigned SPIN_CAP = 1u << 18;

#define STAMP(i)
#define STAMP_DECL
#define STAMP_OUT()
#define TR_DECL
#define TR()
#define TR_OUT()

template <int N> DI void wait_vm() { asm volatile("s_waitcnt vmcnt(%0)" ::"i"(N) : "memory"); }
DI void bar() { asm volatile("s_barrier" ::: "memory"); }
#define HTR_DECL
#define HTR()
#define HTR_OUT()
#define SB() __builtin_amdgcn_sched_barrier(0)
#define SGB(mask, n) __builtin_amdgcn_sched_group_barrier(mask, n, 0)
DI void wait_lgkm0() { __builtin_amdgcn_s_waitcnt(0xC07F); asm volatile("" ::: "memory"); }

DI f32x4 mfma(u32x4 a, u32x4 b, f32x4 c) {
    return __builtin_amdgcn_mfma_f32_16x16x32_bf16(__builtin_bit_cast(s16x8, a), __builtin_bit_cast(s16x8, b), c, 0, 0, 0);
}
DI void mf(f32x4& c, u32x4 a, u32x4 b) { c = mfma(a, b, c); }
template <bool Z> DI void mfz(f32x4& c, u32x4 a, u32x4 b) { c = mfma(a, b, Z ? f32x4{0.f, 0.f, 0.f, 0.f} : c); }
DI int opq(int v) { asm volatile("" : "+v"(v)); return v; }
DI u32x2 trd(LAS char* p) { return __builtin_bit_cast(u32x2, __builtin_amdgcn_ds_read_tr16_b64_v4i16((LAS s16x4*)p)); }
DI void set_lo(u32x4& v, u32x2 x) { v[0] = x[0]; v[1] = x[1]; }
DI void set_hi(u32x4& v, u32x2 x) { v[2] = x[0]; v[3] = x[1]; }
DI u32x4 lds_b128(LAS char* p) { return *(LAS u32x4*)p; }
DI u32x4 lds_tr(LAS char* p, int hi_off) {
    u32x2 lo = __builtin_bit_cast(u32x2, __builtin_amdgcn_ds_read_tr16_b64_v4i16((LAS s16x4*)p));
    u32x2 hi = __builtin_bit_cast(u32x2, __builtin_amdgcn_ds_read_tr16_b64_v4i16((LAS s16x4*)(p + hi_off)));
    return u32x4{lo[0], lo[1], hi[0], hi[1]};
}
DI unsigned pk2(float a, float b) {
    f32x2 v = {a, b};
    return __builtin_bit_cast(unsigned, __builtin_convertvector(v, bf16x2));
}
DI u32x4 pack8(f32x4 a, f32x4 b) { return u32x4{pk2(a[0], a[1]), pk2(a[2], a[3]), pk2(b[0], b[1]), pk2(b[2], b[3])}; }
DI float bflo(unsigned w) { return __uint_as_float(w << 16); }
DI float bfhi(unsigned w) { return __uint_as_float(w & 0xffff0000u); }
DI float xmax(float v) {
    auto a = __builtin_amdgcn_permlane16_swap(__float_as_uint(v), __float_as_uint(v), false, false);
    v = fmaxf(__uint_as_float(a[0]), __uint_as_float(a[1]));
    auto c = __builtin_amdgcn_permlane32_swap(__float_as_uint(v), __float_as_uint(v), false, false);
    return fmaxf(__uint_as_float(c[0]), __uint_as_float(c[1]));
}
DI float xsum(float v) {
    auto a = __builtin_amdgcn_permlane16_swap(__float_as_uint(v), __float_as_uint(v), false, false);
    v = __uint_as_float(a[0]) + __uint_as_float(a[1]);
    auto c = __builtin_amdgcn_permlane32_swap(__float_as_uint(v), __float_as_uint(v), false, false);
    return __uint_as_float(c[0]) + __uint_as_float(c[1]);
}
DI float xsum32(float v) {
    auto c = __builtin_amdgcn_permlane32_swap(__float_as_uint(v), __float_as_uint(v), false, false);
    return __uint_as_float(c[0]) + __uint_as_float(c[1]);
}

struct Frags { u32x4 nat[4]; u32x4 tr[3]; };
template <int I, int N, class F> DI void static_for(F&& f) {
    if constexpr (I < N) { f(std::integral_constant<int, I>{}); static_for<I + 1, N>(f); }
}

DI void dma_p(__amdgpu_buffer_rsrc_t r, unsigned voff, unsigned lds_dst) {
    unsigned keep;
    asm volatile(
        "s_nop 4\n\ts_mov_b32 %0, m0\n\t"
        "s_mov_b32 m0, %3\n\ts_nop 0\n\t"
        "buffer_load_dwordx4 %1, %2, 0 offen sc1 lds\n\t"
        "buffer_load_dwordx4 %1, %2, 0 offen offset:1024 sc1 lds\n\t"
        "buffer_load_dwordx4 %1, %2, 0 offen offset:2048 sc1 lds\n\t"
        "buffer_load_dwordx4 %1, %2, 0 offen offset:3072 sc1 lds\n\t"
        "s_add_u32 m0, m0, 0x1000\n\ts_nop 0\n\t"
        "buffer_load_dwordx4 %1, %2, %4 offen sc1 lds\n\t"
        "buffer_load_dwordx4 %1, %2, %4 offen offset:1024 sc1 lds\n\t"
        "buffer_load_dwordx4 %1, %2, %4 offen offset:2048 sc1 lds\n\t"
        "buffer_load_dwordx4 %1, %2, %4 offen offset:3072 sc1 lds\n\t"
        "s_mov_b32 m0, %0"
        : "=&s"(keep) : "v"(voff), "s"(r), "s"(lds_dst), "s"(4096u) : "memory", "scc");
}

DI bool flags_ok(u32x4 v, unsigned epoch) { return __all(((v[0] ^ epoch) | (v[1] ^ epoch) | (v[2] ^ epoch) | (v[3] ^ epoch)) == 0); }
DI void poll4(__amdgpu_buffer_rsrc_t rws, unsigned off, unsigned epoch) {
#pragma unroll 1
    for (unsigned spin = 0; spin < SPIN_CAP; ++spin) {
        if (flags_ok(__builtin_amdgcn_raw_buffer_load_b128(rws, off, 0, AUX_SC1), epoch)) break;
        __builtin_amdgcn_s_sleep(1);
    }
    __builtin_amdgcn_fence(__ATOMIC_ACQUIRE, "wavefront");
}

}

__global__ __launch_bounds__(512, 2) void mha_fused(const u16* __restrict__ x, const u16* __restrict__ kc,
                                                    const u16* __restrict__ vc, const u16* __restrict__ wq,
                                                    const u16* __restrict__ wo, u16* __restrict__ out,
                                                    char* __restrict__ ws) {
    __shared__ __attribute__((aligned(16))) char smem_[LDS_BYTES];
    LAS char* smem = (LAS char*)smem_;
    const int bid = blockIdx.x;
    const int g = bid & 7, j = (bid >> 3) & 3, b = bid >> 5;
    const int h = 4 * g + j;
    const int wave = __builtin_amdgcn_readfirstlane(threadIdx.x >> 6);
    const int lane = threadIdx.x & 63;
    const int hrot = j;
    const int crot = (b * 11) >> 5 == 0 ? b : ((b * 11) >> 5 == 1 ? b - 3 : b - 6);
    STAMP_DECL
    STAMP(0);

    if (wave >= 4) {
        TR_DECL
        TR();
#define BARL() do { bar(); } while (0)
#define BARH() do { bar(); HTR(); } while (0)
        if (wave < 7) {
            const int lw = wave - 4;
            int ksA = ((b * 10) >> 3) + ((j * 5) >> 1); if (ksA >= 10) ksA -= 10;
            const __amdgpu_buffer_rsrc_t rx = __builtin_amdgcn_make_buffer_rsrc((void*)x, 0, NB * SQ * DM * 2, 0x00020000);
            const __amdgpu_buffer_rsrc_t rq = __builtin_amdgcn_make_buffer_rsrc((void*)wq, 0, DM * HD * 2, 0x00020000);
            const int r8 = lane >> 3, c8 = lane & 7;
            const unsigned sw8 = (unsigned)((c8 ^ r8) * 16);
            const unsigned swt = (unsigned)((2 * ((c8 >> 1) ^ ((r8 >> 1) & 1)) + (c8 & 1)) * 16);
            const unsigned swA = swt ^ (unsigned)((lw & 1) * 64), swB = swA ^ 64u;
            const unsigned vx = (unsigned)(r8 * (DM * 2)) + sw8;
            const unsigned vqA = (unsigned)(r8 * (HD * 2)) + swA, vqB = (unsigned)(r8 * (HD * 2)) + swB;
#define DMA(rs, dst, voff, soff) __builtin_amdgcn_raw_ptr_buffer_load_lds(rs, (LAS void*)(dst), 16, voff, soff, 0, 0)
            LAS char* const sw = smem + lw * 1024;
            const unsigned sx0 = (unsigned)((b * 64 + 8 * lw) * (DM * 2));
            const unsigned sq0 = (unsigned)(8 * lw * (HD * 2) + h * (DH * 2));
            const __amdgpu_buffer_rsrc_t rk = __builtin_amdgcn_make_buffer_rsrc((void*)kc, 0, NB * NH * SK * DH * 2, 0x00020000);
            const unsigned vkv = (unsigned)(r8 * (DH * 2)) + sw8;
            const __amdgpu_buffer_rsrc_t rv = __builtin_amdgcn_make_buffer_rsrc((void*)vc, 0, NB * NH * SK * DH * 2, 0x00020000);
            const __amdgpu_buffer_rsrc_t ro = __builtin_amdgcn_make_buffer_rsrc((void*)wo, 0, HD * DM * 2, 0x00020000);
            auto run = [&](auto THREE) {
                constexpr bool three = decltype(THREE)::value;
                constexpr int NA = three ? 12 : 8, NK = three ? 9 : 6, NBI = three ? 9 : 10;
                static_for<0, 10>([&](auto IT) {
                    constexpr int it = decltype(IT)::value;
                    const unsigned sx = sx0 + ksA * 128, so = sq0 + ksA * 64 * (HD * 2);
                    if (++ksA == 10) ksA = 0;
                    LAS char* sb = sw + (it % 3) * SLOT;
                    SB();
                    if constexpr (it == 0) STAMP(3);
                    if constexpr (it == 1) STAMP(4);
                    if constexpr (it == 2) STAMP(5);
                    if constexpr (it >= 2) {
                        wait_vm<(it == 6 ? NA + NK : NA)>();
                        if constexpr (it == 2) STAMP(6);
                        BARL();
                    }
                    SB();
                    if constexpr (it == 5) {
                        const unsigned sk = (unsigned)(((b * NH + h) * SK + 8 * lw) * (DH * 2));
#pragma unroll
                        for (int rg = 0; rg < (three ? 3 : 2); ++rg)
#pragma unroll
                            for (int st = 0; st < 3; ++st)
                                DMA(rk, smem + L_K + st * 8192 + lw * 1024 + rg * 3072, vkv, sk + st * 128 + rg * 24 * DH * 2);
                    }
#pragma unroll
                    for (int rg = 0; rg < (three ? 3 : 2); ++rg) {
                        DMA(rx, sb + rg * 3072, vx, sx + rg * 24 * DM * 2);
#pragma unroll
                        for (int blk = 0; blk < 3; ++blk)
                            DMA(rq, sb + 8192 + blk * 8192 + rg * 3072, rg == 1 ? vqB : vqA, so + blk * 128 + rg * 24 * HD * 2);
                    }
                });
                const unsigned voA = (unsigned)(r8 * (DM * 2)) + swA, voB = (unsigned)(r8 * (DM * 2)) + swB;
                const unsigned vh = (unsigned)((lane >> 2) * (DM * 2) + ((lane & 3) ^ ((lane >> 4) & 2)) * 16);
                const unsigned colF = (unsigned)(j * 320 + (j & 1) * 64), colH = (unsigned)(j * 320 + ((j & 1) ? 0 : 256));
                int ch = crot;
                static_for<10, 22>([&](auto IT) {
                    constexpr int it = decltype(IT)::value;
                    const int hh = 4 * g + ((hrot + (it - 10) / 3) & 3);
                    const unsigned sv = (unsigned)(((b * NH + hh) * SK + 8 * lw) * (DH * 2) + ch * 128);
                    const unsigned so = (unsigned)((hh * DH + ch * 64) * (DM * 2));
                    if (++ch == 3) ch = 0;
                    LAS char* sb = sw + (it % 3) * SLOT;
                    SB();
                    wait_vm<(it == 10 ? NA : NBI)>();
                    BARL();
                    SB();
#pragma unroll
                    for (int rg = 0; rg < (three ? 3 : 2); ++rg) {
                        DMA(rv, sb + rg * 3072, vkv, sv + rg * 24 * DH * 2);
#pragma unroll
                        for (int blk = 0; blk < 2; ++blk)
                            DMA(ro, sb + 8192 + blk * 8192 + rg * 3072, rg == 1 ? voB : voA, so + colF + blk * 128 + (8 * lw + rg * 24) * DM * 2);
                    }
                    if constexpr (!three) {
#pragma unroll
                        for (int q = 0; q < 4; ++q) DMA(ro, sb - lw * 1024 + 24576 + q * 1024, vh, so + colH + q * 16 * DM * 2);
                    }
                });
                wait_vm<NBI>(); BARL();
                wait_vm<0>(); BARL();
            };
            if (lw < 2) run(std::true_type{}); else run(std::false_type{});
            STAMP(2);
        } else {
            HTR_DECL
            HTR();
            const __amdgpu_buffer_rsrc_t rws = __builtin_amdgcn_make_buffer_rsrc((void*)ws, 0, WS_END, 0x00020000);
            const unsigned epoch_v = __hip_atomic_load((gu32*)(ws + WS_E + b * 128), RLX_AGENT);
#pragma unroll 1
            for (int t = 0; t < 15; ++t) BARH();
            const unsigned epoch = (unsigned)__builtin_amdgcn_readfirstlane(epoch_v) + 1u;
            const int d3 = (lane >> 4) < 3 ? (lane >> 4) : 0;
            const unsigned fo = WS_PF + ((bid & ~24) | (((j + 1 + d3) & 3) << 3)) * 128;
            u32x4 fl = __builtin_amdgcn_raw_buffer_load_b128(rws, fo, 0, AUX_SC1);
            BARH();
            if (!flags_ok(fl, epoch)) poll4(rws, fo, epoch);
            __builtin_amdgcn_fence(__ATOMIC_ACQUIRE, "wavefront");
#pragma unroll 1
            for (int d = 0; d < 3; ++d)
                dma_p(rws, WS_P + ((bid & ~24) | (((j + 1 + d) & 3) << 3)) * 8192 + lane * 16, (unsigned)(L_K + d * 8192));
            BARH();
            wait_vm<0>();
            BARH();
#pragma unroll 1
            for (int t = 18; t < 22; ++t) BARH();
            HTR_OUT();
        }
        TR_OUT();
        STAMP_OUT();
        return;
    }

    const int w = wave;
    const int l15 = lane & 15, lg = lane >> 4;
    const __amdgpu_buffer_rsrc_t rws = __builtin_amdgcn_make_buffer_rsrc((void*)ws, 0, WS_END, 0x00020000);
    const unsigned epoch_v = __hip_atomic_load((gu32*)(ws + WS_E + b * 128), RLX_AGENT);

    const int an0 = l15 * 128 + ((lg ^ (l15 & 7)) << 4);
    const int q4 = l15 >> 2, p4 = l15 & 3;
    const int sq = ((q4 >> 1) & 1) | ((lg & 1) << 1);
    const int kx = (w >> 1) * 4096;
    int aq[2][3], ao[2][3], an[2][2], anA[2][2], anB[2][2];
#pragma unroll
    for (int hi = 0; hi < 2; ++hi) {
#pragma unroll
        for (int u = 0; u < 3; ++u) {
            const int T = 3 * w + u;
            aq[hi][u] = opq(hi * 65536 + 8192 + (T >> 2) * 8192 + (8 * lg + q4) * 128 + (((T & 3) ^ sq) << 5) + p4 * 8);
        }
#pragma unroll
        for (int u = 0; u < 2; ++u) {
            const int T = 2 * w + u;
            ao[hi][u] = opq(hi * 65536 + 8192 + (T >> 2) * 8192 + (8 * lg + q4) * 128 + (((T & 3) ^ sq) << 5) + p4 * 8);
        }
        ao[hi][2] = opq(hi * 65536 + 24576 + (8 * lg + q4) * 64 + (((w & 1) ^ (lg & 1)) << 5) + p4 * 8);
#pragma unroll
        for (int ks = 0; ks < 2; ++ks) {
            an[hi][ks] = opq(hi * 65536 + (an0 ^ (ks << 6)));
            anA[hi][ks] = opq(hi * 65536 + (an0 ^ (ks << 6)) + kx);
            anB[hi][ks] = opq(hi * 65536 + (an0 ^ (ks << 6)) + 4096 - kx);
        }
    }

    int aW[3];
#pragma unroll
    for (int u = 0; u < 3; ++u) {
        const int T = 3 * w + u;
        aW[u] = opq(L_Q + (T >> 2) * 8192 + l15 * 128 + (((2 * (T & 3) + (lg >> 1)) ^ (l15 & 7)) << 4) + (lg & 1) * 8);
    }
    const int aPW = opq(L_POWN + w * 2048 + lane * 16);
    const int aK[2] = {opq(L_K + an0), opq(L_K + (an0 ^ 64))};
    const int aQ[2] = {opq(L_Q + w * 2048 + an0), opq(L_Q + w * 2048 + (an0 ^ 64))};
    const int aP[2] = {opq(L_K + lane * 16 + (w >> 1) * 1024), opq(L_K + lane * 16 + 1024 - (w >> 1) * 1024)};
    f32x4 acc[3][4];
#pragma unroll
    for (int u = 0; u < 3; ++u)
#pragma unroll
        for (int t = 0; t < 4; ++t) { acc[u][t] = f32x4{0.f, 0.f, 0.f, 0.f}; asm volatile("" : "+v"(acc[u][t])); }
#define ILV() do { SGB(0x8, 1); SGB(0x100, 2); SGB(0x8, 1); SGB(0x100, 1); SGB(0x8, 1); SGB(0x100, 1); SGB(0x8, 1); SGB(0x100, 1); SGB(0x8, 1); SGB(0x100, 1); \
                   SGB(0x8, 1); SGB(0x100, 2); SGB(0x8, 1); SGB(0x100, 2); SGB(0x8, 3); } while (0)
#define MA(i) mf(acc[(i) / 4][(i) % 4], c.tr[(i) / 4], c.nat[(i) % 4])
#define LP(reg, off) (smem + (reg)[sl >> 1] + (sl & 1) * SLOT * (1 - (sl >> 1)) + (off))
#define LPU(reg, u, off) (smem + (reg)[sl >> 1][u] + (sl & 1) * SLOT * (1 - (sl >> 1)) + (off))
#define LD_A(X0, X1, X2, X3, X4, X5, X6, X7, X8, X9)                                                        \
        X0; set_lo(n.tr[0], trd(LPU(aq, 0, ks * 4096))); set_hi(n.tr[0], trd(LPU(aq, 0, ks * 4096 + 512))); \
        X1; n.nat[0] = lds_b128(LPU(an, ks, 0));                                                            \
        X2; n.nat[1] = lds_b128(LPU(an, ks, 2048));                                                         \
        X3; n.nat[2] = lds_b128(LPU(an, ks, 4096));                                                         \
        X4; n.nat[3] = lds_b128(LPU(an, ks, 6144));                                                         \
        X5; set_lo(n.tr[1], trd(LPU(aq, 1, ks * 4096))); set_hi(n.tr[1], trd(LPU(aq, 1, ks * 4096 + 512))); \
        X6; set_lo(n.tr[2], trd(LPU(aq, 2, ks * 4096))); set_hi(n.tr[2], trd(LPU(aq, 2, ks * 4096 + 512))); \
        X7; X8; X9;
#define LD_B(X0, X1, X2, X3, X4, X5, X6, X7, X8, X9)                                                        \
        X0; set_lo(n.tr[0], trd(LPU(ao, 0, ks * 4096))); set_hi(n.tr[0], trd(LPU(ao, 0, ks * 4096 + 512))); \
        X1; n.nat[0] = lds_b128(LPU(anA, ks, 0));                                                           \
            set_lo(n.tr[2], trd(LPU(ao, 2, ks * 2048)));                                                    \
        X2; n.nat[1] = lds_b128(LPU(anA, ks, 2048));                                                        \
            set_hi(n.tr[2], trd(LPU(ao, 2, ks * 2048 + 256)));                                              \
        X3; n.nat[2] = lds_b128(LPU(anB, ks, 0));                                                           \
        X4; n.nat[3] = lds_b128(LPU(anB, ks, 2048));                                                        \
        X5; set_lo(n.tr[1], trd(LPU(ao, 1, ks * 4096))); set_hi(n.tr[1], trd(LPU(ao, 1, ks * 4096 + 512))); \
        X6; X7; X8; X9;
    auto grp_AA = [&](Frags& n, const Frags& c, const int sl, const int ks) {
        SB();
        LD_A(MA(0), MA(1), MA(2), MA(3), MA(4), MA(5), MA(6), MA(7), MA(8), MA(9))
        MA(10); MA(11);
        ILV(); SGB(0x8, 2);
        SB();
    };
    auto grp_BA = [&](Frags& n, const Frags& c, const int sl, const int ks) {
        SB();
        LD_B(MA(0), MA(1), MA(2), MA(3), MA(4), MA(5), MA(6), MA(7), MA(8), MA(9))
        MA(10); MA(11);
        ILV(); SGB(0x8, 2);
        SB();
    };
#define MB0 mfz<Z>(acc[0][0], c.nat[0], c.tr[0])
#define MB1 mfz<Z>(acc[0][1], c.nat[1], c.tr[0])
#define MB2 mfz<Z>(acc[2][0], c.nat[0], c.tr[2])
#define MB3 mfz<Z>(acc[2][1], c.nat[1], c.tr[2])
#define MB4 mfz<Z>(acc[0][2], c.nat[2], c.tr[0])
#define MB5 mfz<Z>(acc[0][3], c.nat[3], c.tr[0])
#define MB6 mfz<Z>(acc[1][0], c.nat[0], c.tr[1])
#define MB7 mfz<Z>(acc[1][1], c.nat[1], c.tr[1])
#define MB8 mfz<Z>(acc[1][2], c.nat[2], c.tr[1])
#define MB9 mfz<Z>(acc[1][3], c.nat[3], c.tr[1])
    auto grp_BB = [&](auto ZZ, Frags& n, const Frags& c, const int sl, const int ks) {
        constexpr bool Z = decltype(ZZ)::value;
        SB();
        LD_B(MB0, MB1, MB2, MB3, MB4, MB5, MB6, MB7, MB8, MB9)
        ILV();
        SB();
    };
    auto mma_B = [&](const Frags& c) { constexpr bool Z = false; MB0; MB1; MB2; MB3; MB4; MB5; MB6; MB7; MB8; MB9; };
    using TT = std::true_type; using FF = std::false_type;

    f32x4 oacc[3][4];
#pragma unroll
    for (int u = 0; u < 3; ++u)
#pragma unroll
        for (int t = 0; t < 4; ++t) { oacc[u][t] = f32x4{0.f, 0.f, 0.f, 0.f}; asm volatile("" : "+v"(oacc[u][t])); }
    Frags f0, f1;
#pragma unroll
    for (int t = 0; t < 4; ++t) f1.nat[t] = u32x4{0, 0, 0, 0};
#pragma unroll
    for (int u = 0; u < 3; ++u) f1.tr[u] = u32x4{0, 0, 0, 0};
    static_for<0, 10>([&](auto T_) {
        constexpr int t = decltype(T_)::value;
        bar();
        grp_AA(f0, f1, t % 3, 0);
        grp_AA(f1, f0, t % 3, 1);
        wait_lgkm0();
    });
    STAMP(1);
    const unsigned epoch = (unsigned)__builtin_amdgcn_readfirstlane(epoch_v) + 1u;
    u32x4 vw[5];
    auto cvt_vw = [&]() {
#pragma unroll
        for (int u = 0; u < 2; ++u) {
            vw[2 * u] = pack8(acc[u][0], acc[u][1]);
            vw[2 * u + 1] = pack8(acc[u][2], acc[u][3]);
        }
        vw[4] = pack8(acc[2][0], acc[2][1]);
    };
    auto load_P = [&](u32x4 (&pf)[4], int m, int p) {
        LAS char* pb = smem + aP[p] + (m == 0 ? L_POWN - L_K : (m - 1) * 8192);
#pragma unroll
        for (int t = 0; t < 4; ++t) pf[t] = lds_b128(pb + t * 2048);
    };
    auto mma_C = [&](const u32x4 (&pf)[4], int p) {
#pragma unroll
        for (int t = 0; t < 4; ++t) {
            mf(oacc[0][t], vw[p], pf[t]);
            mf(oacc[1][t], vw[2 + p], pf[t]);
            if (p == 0) mf(oacc[2][t], vw[4], pf[t]);
        }
    };
    {
        constexpr int sl = 10 % 3;
        bar();
        grp_BA(f0, f1, sl, 0);
        u32x2 qv[3][4];
#pragma unroll
        for (int u = 0; u < 3; ++u)
#pragma unroll
            for (int t = 0; t < 4; ++t) qv[u][t] = u32x2{pk2(acc[u][t][0], acc[u][t][1]), pk2(acc[u][t][2], acc[u][t][3])};
        {
            constexpr bool Z = true;
            Frags& n = f1; const Frags& c = f0; const int ks = 1;
            LD_B(MB0, MB1, MB2, MB3, MB4, MB5, MB6, MB7, MB8, MB9)
        }
#pragma unroll
        for (int u = 0; u < 3; ++u) {
            LAS char* qb = smem + aW[u];
#pragma unroll
            for (int t = 0; t < 4; ++t) *(LAS u32x2*)(qb + t * 2048) = qv[u][t];
        }
        SGB(0x2, 24);
#pragma unroll
        for (int i = 0; i < 10; ++i) { SGB(0x8, 1); SGB(0x100, 1); SGB(0x200, 1); }
        SGB(0x200, 2);
        SB();
        wait_lgkm0();
    }
    STAMP(2);
    {
        constexpr int sl = 11 % 3;
        bar();
        SB();
        f32x4 s[4];
        u32x4 kf[2][4], qf[2];
        auto ld_kq = [&](int k6) {
            const int o = (k6 >> 1) * 8192;
            qf[k6 & 1] = lds_b128(smem + aQ[k6 & 1] + o);
#pragma unroll
            for (int t = 0; t < 4; ++t) kf[k6 & 1][t] = lds_b128(smem + aK[k6 & 1] + o + t * 2048);
        };
        ld_kq(0);
        ld_kq(1);
        {
            constexpr bool Z = false; const Frags& c = f1;
            MB0; MB1; MB2; MB3; MB4; MB5; MB6; MB7; MB8; MB9;
        }
        SB();
#pragma unroll
        for (int k6 = 0; k6 < 6; ++k6) {
#pragma unroll
            for (int t = 0; t < 4; ++t) s[t] = mfma(kf[k6 & 1][t], qf[k6 & 1], k6 == 0 ? f32x4{0.f, 0.f, 0.f, 0.f} : s[t]);
            SB();
            if (k6 + 2 < 6) ld_kq(k6 + 2);
            else if (k6 == 4) { Frags& n = f0; const int ks = 0; LD_B(, , , , , , , , , ) }
            SB();
        }
        float mx = fmaxf(fmaxf(fmaxf(s[0][0], s[0][1]), fmaxf(s[0][2], s[0][3])), fmaxf(fmaxf(s[1][0], s[1][1]), fmaxf(s[1][2], s[1][3])));
        mx = fmaxf(mx, fmaxf(fmaxf(fmaxf(s[2][0], s[2][1]), fmaxf(s[2][2], s[2][3])), fmaxf(fmaxf(s[3][0], s[3][1]), fmaxf(s[3][2], s[3][3]))));
        mx = xmax(mx);
        constexpr float C = 0.07216878364870322f * 1.4426950408889634f;
        const float mc = mx * C;
#pragma unroll
        for (int t = 0; t < 4; ++t)
#pragma unroll
            for (int i = 0; i < 4; ++i) s[t][i] = __builtin_amdgcn_exp2f(fmaf(s[t][i], C, -mc));
        const f32x4 s01 = s[0] + s[1], s23 = s[2] + s[3], s4 = s01 + s23;
        const float rs = __builtin_amdgcn_rcpf(xsum((s4[0] + s4[1]) + (s4[2] + s4[3])));
        const u32x4 pf0 = pack8(s[0] * rs, s[1] * rs), pf1 = pack8(s[2] * rs, s[3] * rs);
        {
            constexpr bool Z = false; Frags& n = f1; const Frags& c = f0; const int ks = 1;
            LD_B(MB0, MB1, MB2, MB3, MB4, MB5, MB6, MB7, MB8, MB9)
        }
        __builtin_amdgcn_raw_buffer_store_b128(pf0, rws, WS_P + bid * 8192 + w * 2048 + lane * 16, 0, AUX_SC1);
        __builtin_amdgcn_raw_buffer_store_b128(pf1, rws, WS_P + bid * 8192 + w * 2048 + 1024 + lane * 16, 0, AUX_SC1);
        *(LAS u32x4*)(smem + aPW) = pf0;
        *(LAS u32x4*)(smem + aPW + 1024) = pf1;
#pragma unroll
        for (int i = 0; i < 10; ++i) { SGB(0x8, 1); SGB(0x100, 1); SGB(0x2, 9); }
        SB();
        wait_lgkm0();
    }
    auto stage = [&](auto S_) {
        constexpr int s = decltype(S_)::value;
        constexpr int m = s / 3, c3 = s % 3;
        constexpr bool doC = m >= 1 && c3 != 0;
        constexpr int sl = (10 + s) % 3;
        bar();
        grp_BB(FF{}, f0, f1, sl, 0);
        if constexpr (c3 == 0) cvt_vw();
        u32x4 pf[4];
        {
            constexpr bool Z = c3 == 0; Frags& n = f1; const Frags& c = f0; const int ks = 1;
            SB();
            LD_B(MB0, MB1, MB2, MB3, MB4, MB5, MB6, MB7, MB8, MB9)
            if constexpr (doC) { load_P(pf, m - 1, c3 - 1); mma_C(pf, c3 - 1); }
            ILV();
            if constexpr (doC) { }
            SB();
        }
        if constexpr (s == 2) {
            wait_vm<0>();
            if (lane == 0) __hip_atomic_store((gu32*)(ws + WS_PF + bid * 128 + w * 4), epoch, RLX_AGENT);
        }
        wait_lgkm0();
    };
    static_for<2, 12>(stage);
    STAMP(3);
    mma_B(f1);
    {
        u32x4 pa[4], pb[4];
        load_P(pa, 3, 0);
        load_P(pb, 3, 1);
        cvt_vw();
        mma_C(pa, 0);
        mma_C(pb, 1);
    }
    STAMP(4);

    {
        const unsigned ob = WS_IN + (((bid & ~7) | (l15 >> 2)) * 8 + g) * INB + (lg * 4 + (l15 & 3)) * 16;
#pragma unroll
        for (int u = 0; u < 3; ++u)
#pragma unroll
            for (int m = 0; m < 2; ++m)
                __builtin_amdgcn_raw_buffer_store_b128(pack8(oacc[u][2 * m], oacc[u][2 * m + 1]), rws,
                                                       ob + m * (4 * 8 * INB) + (u < 2 ? 2 * w + u : 8 + w) * 256, 0, AUX_SC1);
        STAMP(5);
        wait_vm<0>();
        STAMP(6);
        if (lane == 0) __hip_atomic_store((gu32*)(ws + WS_OF + bid * 128 + w * 4), epoch, RLX_AGENT);
        if (w == 0) poll4(rws, WS_OF + ((bid & ~7) | (lane & 7)) * 128, epoch);
        STAMP(7);
        bar();
        if (w < 3) {
            const int T = w * 64 + lane;
            u32x4 v[8];
#pragma unroll
            for (int s = 0; s < 8; ++s)
                v[s] = __builtin_amdgcn_raw_buffer_load_b128(rws, WS_IN + (bid * 8 + s) * INB + T * 16, 0, AUX_SC1);
            float r[8];
#pragma unroll
            for (int i = 0; i < 4; ++i) { r[2 * i] = bflo(v[0][i]); r[2 * i + 1] = bfhi(v[0][i]); }
#pragma unroll
            for (int s = 1; s < 8; ++s)
#pragma unroll
                for (int i = 0; i < 4; ++i) { r[2 * i] += bflo(v[s][i]); r[2 * i + 1] += bfhi(v[s][i]); }
            if (w == 2) {
#pragma unroll
                for (int i = 0; i < 8; ++i) r[i] = xsum32(r[i]);
            }
            if (T < 160) {
                const int U = T >> 4, gg = (T >> 2) & 3, qq = T & 3;
                const int row = 32 * (g >> 2) + 4 * (g & 3) + qq;
                const int col = (j & 1) ? (U < 8 ? 32 + 16 * U : 16 * (U - 8)) : 16 * U;
                u16* o = out + (b * SQ + row) * DM + j * 160 + col + 4 * gg;
                *(u32x2*)o = u32x2{pk2(r[0], r[1]), pk2(r[2], r[3])};
                *(u32x2*)(o + 16 * DM) = u32x2{pk2(r[4], r[5]), pk2(r[6], r[7])};
            }
        }
        STAMP(8);
        STAMP(9);
        STAMP_OUT();
        if (g == 0 && j == 0 && threadIdx.x == 0) __hip_atomic_store((gu32*)(ws + WS_E + b * 128), epoch, RLX_AGENT);
    }
}

extern "C" void kernel_launch(void* const* d_in, const int* in_sizes, int n_in, void* d_out, int out_size,
                              void* d_ws, size_t ws_size, hipStream_t stream) {
    mha_fused<<<256, 512, 0, stream>>>((const u16*)d_in[0], (const u16*)d_in[1], (const u16*)d_in[2],
                                       (const u16*)d_in[3], (const u16*)d_in[4], (u16*)d_out, (char*)d_ws);
}
```

```cpp
#define AUX_K 2
#define PF_B1 40
#include <hip/hip_runtime.h>
#include <stdint.h>
#include <type_traits>

#define LAS __attribute__((address_space(3)))
#define GAS __attribute__((address_space(1)))
typedef unsigned short u16;
typedef short s16x4 __attribute__((ext_vector_type(4)));
typedef short s16x8 __attribute__((ext_vector_type(8)));
typedef float f32x4 __attribute__((ext_vector_type(4)));
typedef float f32x2 __attribute__((ext_vector_type(2)));
typedef __bf16 bf16x2 __attribute__((ext_vector_type(2)));
typedef unsigned u32x4 __attribute__((ext_vector_type(4)));
typedef unsigned u32x2 __attribute__((ext_vector_type(2)));
typedef GAS unsigned gu32;
#define RLX_AGENT __ATOMIC_RELAXED, __HIP_MEMORY_SCOPE_AGENT
#define DI __device__ __forceinline__

namespace {

constexpr int NB = 8, SQ = 64, DM = 640, NH = 32, SK = 64, DH = 192, HD = NH * DH;
constexpr int AUX_SC1 = 16;
#define AUX_WQ 0
#define AUX_WO 0
#define AUX_X 0
#define AUX_V 0

constexpr int SLOT = 32768;
constexpr int L_K = 3 * SLOT;
constexpr int L_I10 = L_K + 24576;
constexpr int L_Q = 1 * SLOT;
constexpr int L_POWN = L_I10 + 28672;
constexpr int LDS_BYTES = 163840;

constexpr unsigned WS_E = 0;
constexpr unsigned WS_OF = 4096 + 256 * 128;
constexpr unsigned WS_RDY = 4096;
constexpr unsigned WS_X = 131072;
constexpr unsigned INB = 3072;
constexpr unsigned XCH = 8192 + 8 * INB;
constexpr unsigned WS_END = WS_X + 256 * XCH;
constexpr unsigned SPIN_CAP = 1u << 16;
#define TAIL_SLEEP 8

#define STAMP(i)
#define STAMP_DECL
#define STAMP_OUT()
#define TR_DECL
#define TR()
#define TR_OUT()

template <int N> DI void wait_vm() { asm volatile("s_waitcnt vmcnt(%0)" ::"i"(N) : "memory"); }
DI void bar() { asm volatile("s_barrier" ::: "memory"); }
#define HTR_DECL
#define HTR()
#define HTR_OUT()
#define SB() __builtin_amdgcn_sched_barrier(0)
#define SGB(mask, n) __builtin_amdgcn_sched_group_barrier(mask, n, 0)
DI void wait_lgkm0() { __builtin_amdgcn_s_waitcnt(0xC07F); asm volatile("" ::: "memory"); }

DI f32x4 mfma(u32x4 a, u32x4 b, f32x4 c) {
    return __builtin_amdgcn_mfma_f32_16x16x32_bf16(__builtin_bit_cast(s16x8, a), __builtin_bit_cast(s16x8, b), c, 0, 0, 0);
}
DI void mf(f32x4& c, u32x4 a, u32x4 b) { c = mfma(a, b, c); }
template <bool Z> DI void mfz(f32x4& c, u32x4 a, u32x4 b) { c = mfma(a, b, Z ? f32x4{0.f, 0.f, 0.f, 0.f} : c); }
DI int opq(int v) { asm volatile("" : "+v"(v)); return v; }
DI u32x2 trd(LAS char* p) { return __builtin_bit_cast(u32x2, __builtin_amdgcn_ds_read_tr16_b64_v4i16((LAS s16x4*)p)); }
DI void set_lo(u32x4& v, u32x2 x) { v[0] = x[0]; v[1] = x[1]; }
DI void set_hi(u32x4& v, u32x2 x) { v[2] = x[0]; v[3] = x[1]; }
DI u32x4 lds_b128(LAS char* p) { return *(LAS u32x4*)p; }
DI u32x4 lds_tr(LAS char* p, int hi_off) {
    u32x2 lo = __builtin_bit_cast(u32x2, __builtin_amdgcn_ds_read_tr16_b64_v4i16((LAS s16x4*)p));
    u32x2 hi = __builtin_bit_cast(u32x2, __builtin_amdgcn_ds_read_tr16_b64_v4i16((LAS s16x4*)(p + hi_off)));
    return u32x4{lo[0], lo[1], hi[0], hi[1]};
}
DI unsigned pk2(float a, float b) {
    f32x2 v = {a, b};
    return __builtin_bit_cast(unsigned, __builtin_convertvector(v, bf16x2));
}
DI u32x4 pack8(f32x4 a, f32x4 b) { return u32x4{pk2(a[0], a[1]), pk2(a[2], a[3]), pk2(b[0], b[1]), pk2(b[2], b[3])}; }
DI float bflo(unsigned w) { return __uint_as_float(w << 16); }
DI float bfhi(unsigned w) { return __uint_as_float(w & 0xffff0000u); }
DI float xmax(float v) {
    auto a = __builtin_amdgcn_permlane16_swap(__float_as_uint(v), __float_as_uint(v), false, false);
    v = fmaxf(__uint_as_float(a[0]), __uint_as_float(a[1]));
    auto c = __builtin_amdgcn_permlane32_swap(__float_as_uint(v), __float_as_uint(v), false, false);
    return fmaxf(__uint_as_float(c[0]), __uint_as_float(c[1]));
}
DI float xsum(float v) {
    auto a = __builtin_amdgcn_permlane16_swap(__float_as_uint(v), __float_as_uint(v), false, false);
    v = __uint_as_float(a[0]) + __uint_as_float(a[1]);
    auto c = __builtin_amdgcn_permlane32_swap(__float_as_uint(v), __float_as_uint(v), false, false);
    return __uint_as_float(c[0]) + __uint_as_float(c[1]);
}
DI float xsum32(float v) {
    auto c = __builtin_amdgcn_permlane32_swap(__float_as_uint(v), __float_as_uint(v), false, false);
    return __uint_as_float(c[0]) + __uint_as_float(c[1]);
}

struct Frags { u32x4 nat[4]; u32x4 tr[3]; };
template <int I, int N, class F> DI void static_for(F&& f) {
    if constexpr (I < N) { f(std::integral_constant<int, I>{}); static_for<I + 1, N>(f); }
}

DI unsigned sload(const char* p) { unsigned v; asm volatile("s_load_dword %0, %1, 0x0\n\ts_waitcnt lgkmcnt(0)" : "=s"(v) : "s"(p) : "memory"); return v; }
DI void gld(u32x4& d, __amdgpu_buffer_rsrc_t r, unsigned voff, unsigned soff) {
    asm volatile("buffer_load_dwordx4 %0, %1, %2, %3 offen" : "=v"(d) : "v"(voff), "s"(r), "s"(soff) : "memory");
}
DI void gld_sc1(u32x4& d, __amdgpu_buffer_rsrc_t r, unsigned voff) {
    asm volatile("buffer_load_dwordx4 %0, %1, %2, 0 offen sc1" : "=v"(d) : "v"(voff), "s"(r) : "memory");
}
template <int AUX> DI void dma1(__amdgpu_buffer_rsrc_t r, unsigned lds_dst, unsigned voff, unsigned soff) {
    if (AUX == 2) asm volatile("s_mov_b32 m0, %0\n\ts_nop 0\n\tbuffer_load_dwordx4 %1, %2, %3 offen nt lds" :: "s"(lds_dst), "v"(voff), "s"(r), "s"(soff) : "memory");
    else asm volatile("s_mov_b32 m0, %0\n\ts_nop 0\n\tbuffer_load_dwordx4 %1, %2, %3 offen lds" :: "s"(lds_dst), "v"(voff), "s"(r), "s"(soff) : "memory");
}
template <int N> DI void wait_vm_for(u32x4& a) { asm volatile("s_waitcnt vmcnt(%1)" : "+v"(a) : "i"(N) : "memory"); }
DI void landed(u32x4& a) { asm volatile("" : "+v"(a) :: "memory"); }

#define PF_LO 2
#define PF_SPAN 6
constexpr int PF_N = 52;
static_assert(PF_LO + PF_SPAN - 1 <= 8 && PF_B1 >= 28, "");
#define P_ISSUE 15
constexpr int b_region(int it) { return it == 10 ? 4 : (it == 11 ? 2 : (it == 12 ? 0 : ((it - 13) % 4 == 0 ? 4 : ((it - 13) % 4 == 1 ? 1 : ((it - 13) % 4 == 2 ? 2 : 0))))); }
constexpr int region_base(int r) { return r == 4 ? L_I10 : r * SLOT; }

constexpr unsigned EMPTY = 0xffffffffu;
DI u32x4 not_empty(u32x4 v) { return u32x4{min(v[0], EMPTY - 1), min(v[1], EMPTY - 1), min(v[2], EMPTY - 1), min(v[3], EMPTY - 1)}; }
DI unsigned umax4(u32x4 v) { return max(max(v[0], v[1]), max(v[2], v[3])); }

}

__global__ __launch_bounds__(512, 2) void mha_fused(const u16* __restrict__ x, const u16* __restrict__ kc,
                                                    const u16* __restrict__ vc, const u16* __restrict__ wq,
                                                    const u16* __restrict__ wo, u16* __restrict__ out,
                                                    char* __restrict__ ws) {
    __shared__ __attribute__((aligned(16))) char smem_[LDS_BYTES];
    LAS char* smem = (LAS char*)smem_;
    const int bid = blockIdx.x;
    const int g = bid & 7, j = (bid >> 3) & 3, b = bid >> 5;
    const int h = 4 * g + j;
    const int wave = __builtin_amdgcn_readfirstlane(threadIdx.x >> 6);
    const int lane = threadIdx.x & 63;
    int ksA0 = ((b * 10) >> 3) + ((j * 5) >> 1); if (ksA0 >= 10) ksA0 -= 10;
    const int hrot = (j + b) & 3;
    const int crot = b >> 2;
    STAMP_DECL
    STAMP(0);
    const int r8 = lane >> 3, c8 = lane & 7;
    const unsigned sw8 = (unsigned)((c8 ^ r8) * 16);
    const unsigned swt = (unsigned)((2 * ((c8 >> 1) ^ ((r8 >> 1) & 1)) + (c8 & 1)) * 16);
    const unsigned vx = (unsigned)(r8 * (DM * 2)) + sw8;
    const __amdgpu_buffer_rsrc_t rx = __builtin_amdgcn_make_buffer_rsrc((void*)x, 0, NB * SQ * DM * 2, 0x00020000);
    const __amdgpu_buffer_rsrc_t rq = __builtin_amdgcn_make_buffer_rsrc((void*)wq, 0, DM * HD * 2, 0x00020000);
#define DMAX(rs, dst, voff, soff, aux) __builtin_amdgcn_raw_ptr_buffer_load_lds(rs, (LAS void*)(dst), 16, voff, soff, 0, aux)
#define AUXOF_rq AUX_WQ
#define AUXOF_rx AUX_X
#define AUXOF_rv AUX_V
#define AUXOF_ro AUX_WO
#define DMA(rs, dst, voff, soff) DMAX(rs, dst, voff, soff, AUXOF_##rs)
    {
        const unsigned vq = (unsigned)(r8 * (HD * 2)) + (swt ^ (unsigned)((wave & 1) * 64));
        const unsigned sx = (unsigned)((b * 64 + 8 * wave) * (DM * 2)), so = (unsigned)(8 * wave * (HD * 2) + h * (DH * 2));
#pragma unroll
        for (int it = 0; it < 2; ++it) {
            LAS char* sb = smem + it * SLOT + wave * 1024;
            const int ks = ksA0 + it == 10 ? 0 : ksA0 + it;
#pragma unroll
            for (int blk = 0; blk < 3; ++blk) DMA(rq, sb + 8192 + blk * 8192, vq, so + ks * 64 * (HD * 2) + blk * 128);
            DMA(rx, sb, vx, sx + ks * 128);
        }
    }
    STAMP(3);

    if (wave >= 4) {
        TR_DECL
#define BARL() do { bar(); } while (0)
#define BARH() do { bar(); HTR(); } while (0)
        const __amdgpu_buffer_rsrc_t rv = __builtin_amdgcn_make_buffer_rsrc((void*)vc, 0, NB * NH * SK * DH * 2, 0x00020000);
        const __amdgpu_buffer_rsrc_t ro = __builtin_amdgcn_make_buffer_rsrc((void*)wo, 0, HD * DM * 2, 0x00020000);
        const unsigned vkv = (unsigned)(r8 * (DH * 2)) + sw8;
        const unsigned vh = (unsigned)((lane >> 2) * (DM * 2) + ((lane & 3) ^ ((lane >> 4) & 2)) * 16);
        const unsigned colF = (unsigned)(j * 320 + (j & 1) * 64), colH = (unsigned)(j * 320 + ((j & 1) ? 0 : 256));
        auto stage_hh = [&](int s) { return 4 * g + ((hrot + s / 3) & 3); };
        auto stage_ch = [&](int s) { int c = s % 3 + crot; return c >= 3 ? c - 3 : c; };
        const __amdgpu_buffer_rsrc_t rws = __builtin_amdgcn_make_buffer_rsrc((void*)ws, 0, WS_END, 0x00020000);
        if (wave < 7) {
            const int lw = wave - 4;
            const unsigned swA = swt ^ (unsigned)((lw & 1) * 64), swB = swA ^ 64u;
            const unsigned vqA = (unsigned)(r8 * (HD * 2)) + swA, vqB = (unsigned)(r8 * (HD * 2)) + swB;
            const unsigned voA = (unsigned)(r8 * (DM * 2)) + swA, voB = (unsigned)(r8 * (DM * 2)) + swB;
            LAS char* const sw = smem + lw * 1024;
            const unsigned sx0 = (unsigned)((b * 64 + 8 * lw) * (DM * 2));
            const unsigned sq0 = (unsigned)(8 * lw * (HD * 2) + h * (DH * 2));
            auto run = [&](auto THREE) {
                constexpr bool three = decltype(THREE)::value;
                constexpr int NA = three ? 12 : 8, NBI = three ? 9 : 10, NP = three ? 3 : 2;
                wait_vm<4>();
                static_for<2, 10>([&](auto IT) {
                    constexpr int it = decltype(IT)::value;
                    int ks = ksA0 + it; if (ks >= 10) ks -= 10;
                    const unsigned sx = sx0 + ks * 128, so = sq0 + ks * 64 * (HD * 2);
                    LAS char* sb = sw + (it % 3) * SLOT;
                    SB();
                    if constexpr (it > 2) wait_vm<NA>();
                    BARL();
                    SB();
#pragma unroll
                    for (int rg = 0; rg < (three ? 3 : 2); ++rg) {
                        DMA(rx, sb + rg * 3072, vx, sx + rg * 24 * DM * 2);
#pragma unroll
                        for (int blk = 0; blk < 3; ++blk)
                            DMA(rq, sb + 8192 + blk * 8192 + rg * 3072, rg == 1 ? vqB : vqA, so + blk * 128 + rg * 24 * HD * 2);
                    }
                });
                wait_vm<NA>(); BARL();
                static_for<11, 22>([&](auto IT) {
                    constexpr int it = decltype(IT)::value;
                    const int hh = stage_hh(it - 10), ch = stage_ch(it - 10);
                    const unsigned sv = (unsigned)(((b * NH + hh) * SK + 8 * lw) * (DH * 2) + ch * 128);
                    const unsigned so = (unsigned)((hh * DH + ch * 64) * (DM * 2));
                    LAS char* sb = sw + region_base(b_region(it));
                    SB();
                    if constexpr (it == 11) wait_vm<0>();
                    else if constexpr (it > 12) {
                        if constexpr (it - 1 >= 17 && it - 1 < 21) { if (((b + it - 18) & 3) != 0) wait_vm<NBI + NP>(); else wait_vm<NBI>(); }
                        else wait_vm<NBI>();
                    }
                    BARL();
                    SB();
#pragma unroll
                    for (int rg = 0; rg < (three ? 3 : 2); ++rg) {
                        DMA(rv, sb + rg * 3072, vkv, sv + rg * 24 * DH * 2);
#pragma unroll
                        for (int blk = 0; blk < 2; ++blk)
                            DMA(ro, sb + 8192 + blk * 8192 + rg * 3072, rg == 1 ? voB : voA, so + colF + blk * 128 + (8 * lw + rg * 24) * DM * 2);
                    }
                    if constexpr (!three) {
#pragma unroll
                        for (int q = 0; q < 4; ++q) DMA(ro, sb - lw * 1024 + 24576 + q * 1024, vh, so + colH + q * 16 * DM * 2);
                    }
                    if constexpr (it >= 17 && it < 21) {
                        const int d = (b + it - 17) & 3;
                        if (d != 0) {
                            const unsigned src = WS_X + (unsigned)(((bid & ~24) | (((j + d) & 3) << 3)) * XCH);
#pragma unroll
                            for (int i = 0; i < NP; ++i)
                                __builtin_amdgcn_raw_ptr_buffer_load_lds(rws, (LAS void*)(smem + L_K + (d - 1) * 8192 + (lw + 3 * i) * 1024), 16,
                                                                         (unsigned)(lane * 16), src + (lw + 3 * i) * 1024, 0, AUX_SC1);
                        }
                    }
                });
                wait_vm<NBI>(); BARL();
                wait_vm<0>(); BARL();
            };
            if (lw < 2) run(std::true_type{}); else run(std::false_type{});
            STAMP(2);
        } else {
            HTR_DECL
            const __amdgpu_buffer_rsrc_t rk = __builtin_amdgcn_make_buffer_rsrc((void*)kc, 0, NB * NH * SK * DH * 2, 0x00020000);
            const unsigned vo0 = (unsigned)(r8 * (DM * 2)) + swt, vo1 = (unsigned)(r8 * (DM * 2)) + (swt ^ 64u);
            auto piece_load = [&](auto N_) {
                constexpr int n = decltype(N_)::value;
                if constexpr (n < 28) {
                    const int hh = stage_hh(0), ch = stage_ch(0);
                    const unsigned so = (unsigned)((hh * DH + ch * 64) * (DM * 2));
                    constexpr unsigned dst = L_I10 + n * 1024;
                    if constexpr (n < 8) dma1<AUX_V>(rv, dst, vkv, (unsigned)(((b * NH + hh) * SK + n * 8) * (DH * 2) + ch * 128));
                    else if constexpr (n < 24) dma1<AUX_WO>(ro, dst, (n & 1) ? vo1 : vo0, so + colF + ((n - 8) >> 3) * 128 + (n & 7) * 8 * (DM * 2));
                    else dma1<AUX_WO>(ro, dst, vh, so + colH + (n - 24) * 16 * (DM * 2));
                } else {
                    constexpr int st = (n - 28) / 8, rg = (n - 28) % 8;
                    dma1<AUX_K>(rk, (unsigned)(L_K + st * 8192 + rg * 1024), vkv, (unsigned)(((b * NH + h) * SK + rg * 8) * (DH * 2) + st * 128));
                }
            };
            const unsigned epoch = sload(ws + WS_E + b * 128) + 1u;
            HTR();
            wait_vm<4>();
            const int tb = PF_LO + (bid >> 3) % PF_SPAN;
            const u32x4 e4 = {EMPTY, EMPTY, EMPTY, EMPTY};
            BARH();
            wait_vm<0>();
            BARH();
#pragma unroll 1
            for (int t = 2; t < 9; ++t) {
                BARH();
                if (t == tb) {
                    static_for<0, PF_B1>(piece_load);
#pragma unroll
                    for (int i = 0; i < 8; ++i)
                        __builtin_amdgcn_raw_buffer_store_b128(e4, rws, WS_X + bid * XCH + i * 1024 + lane * 16, 0, AUX_SC1);
                }
            }
            BARH();
            static_for<PF_B1, 52>(piece_load);
            wait_vm<52 - PF_B1>();
            if (lane == 0) __hip_atomic_store((gu32*)(ws + WS_RDY + bid * 128), epoch, RLX_AGENT);
            const int peer = (bid & ~24) | (((j + 1 + (lane & 3)) & 3) << 3);
            unsigned rdy;
            const int aPH = opq(L_K + lane * 16);
            static_for<10, 22>([&](auto T_) {
                constexpr int t = decltype(T_)::value;
                BARH();
                if constexpr (t == 10) wait_vm<0>();
                if constexpr (t == 13) {
                    asm volatile("buffer_load_dword %0, %1, %2, 0 offen sc1" : "=v"(rdy) : "v"(WS_RDY + peer * 128), "s"(rws) : "memory");
                }
                if constexpr (t == 14) {
                    asm volatile("s_waitcnt vmcnt(0)" : "+v"(rdy) :: "memory");
#pragma unroll 1
                    for (unsigned spin = 0; spin < SPIN_CAP && !__all(rdy == epoch); ++spin) {
                        __builtin_amdgcn_s_sleep(1);
                        asm volatile("buffer_load_dword %0, %1, %2, 0 offen sc1\n\ts_waitcnt vmcnt(0)" : "=&v"(rdy) : "v"(WS_RDY + peer * 128), "s"(rws) : "memory");
                    }
                }
                if constexpr (t >= 17 && t < 21) {
                    constexpr int m = t - 17;
                    const int d = (b + m) & 3;
                    if (d != 0) {
                        unsigned mx = 0;
#pragma unroll
                        for (int i = 0; i < 8; ++i) mx = max(mx, umax4(*(LAS u32x4*)(smem + aPH + (d - 1) * 8192 + i * 1024)));
                        if (!__all(mx != EMPTY)) {
                            u32x4 p[8];
#pragma unroll 1
                            for (unsigned spin = 0; spin < SPIN_CAP; ++spin) {
#pragma unroll
                                for (int i = 0; i < 8; ++i)
                                    gld_sc1(p[i], rws, WS_X + ((bid & ~24) | (((j + d) & 3) << 3)) * XCH + i * 1024 + lane * 16);
                                wait_vm<0>();
                                mx = 0;
#pragma unroll
                                for (int i = 0; i < 8; ++i) { landed(p[i]); mx = max(mx, umax4(p[i])); }
                                if (__all(mx != EMPTY)) break;
                            }
#pragma unroll
                            for (int i = 0; i < 8; ++i) *(LAS u32x4*)(smem + aPH + (d - 1) * 8192 + i * 1024) = p[i];
                        }
                    }
                }
                wait_lgkm0();
            });
            HTR_OUT();
        }
        TR_OUT();
        STAMP_OUT();
        return;
    }

    const int w = wave;
    const int l15 = lane & 15, lg = lane >> 4;
    const __amdgpu_buffer_rsrc_t rws = __builtin_amdgcn_make_buffer_rsrc((void*)ws, 0, WS_END, 0x00020000);
    const unsigned epoch = sload(ws + WS_E + b * 128) + 1u;

    const int an0 = l15 * 128 + ((lg ^ (l15 & 7)) << 4);
    const int q4 = l15 >> 2, p4 = l15 & 3;
    const int sq = ((q4 >> 1) & 1) | ((lg & 1) << 1);
    const int kx = (w >> 1) * 4096;
    int aq[2][3], ao[3][3], an[2][2], anA[3][2], anB[3][2];
#pragma unroll
    for (int hi = 0; hi < 3; ++hi) {
        const int base = hi == 2 ? L_I10 : hi * 65536;
        if (hi < 2) {
#pragma unroll
            for (int u = 0; u < 3; ++u) {
                const int T = 3 * w + u;
                aq[hi][u] = opq(base + 8192 + (T >> 2) * 8192 + (8 * lg + q4) * 128 + (((T & 3) ^ sq) << 5) + p4 * 8);
            }
            an[hi][0] = opq(base + an0);
            an[hi][1] = opq(base + (an0 ^ 64));
        }
#pragma unroll
        for (int u = 0; u < 2; ++u) {
            const int T = 2 * w + u;
            ao[hi][u] = opq(base + 8192 + (T >> 2) * 8192 + (8 * lg + q4) * 128 + (((T & 3) ^ sq) << 5) + p4 * 8);
        }
        ao[hi][2] = opq(base + 24576 + (8 * lg + q4) * 64 + (((w & 1) ^ (lg & 1)) << 5) + p4 * 8);
#pragma unroll
        for (int ks = 0; ks < 2; ++ks) {
            anA[hi][ks] = opq(base + (an0 ^ (ks << 6)) + kx);
            anB[hi][ks] = opq(base + (an0 ^ (ks << 6)) + 4096 - kx);
        }
    }

    int aW[3];
#pragma unroll
    for (int u = 0; u < 3; ++u) {
        const int T = 3 * w + u;
        aW[u] = opq(L_Q + (T >> 2) * 8192 + l15 * 128 + (((2 * (T & 3) + (lg >> 1)) ^ (l15 & 7)) << 4) + (lg & 1) * 8);
    }
    const int aPW = opq(L_POWN + w * 2048 + lane * 16);
    const int aK[2] = {opq(L_K + an0), opq(L_K + (an0 ^ 64))};
    const int aQ[2] = {opq(L_Q + w * 2048 + an0), opq(L_Q + w * 2048 + (an0 ^ 64))};
    const int aP[2] = {opq(L_K + lane * 16 + (w >> 1) * 1024), opq(L_K + lane * 16 + 1024 - (w >> 1) * 1024)};
    f32x4 acc[3][4];
#pragma unroll
    for (int u = 0; u < 3; ++u)
#pragma unroll
        for (int t = 0; t < 4; ++t) { acc[u][t] = f32x4{0.f, 0.f, 0.f, 0.f}; asm volatile("" : "+v"(acc[u][t])); }
#define ILV() do { SGB(0x8, 1); SGB(0x100, 2); SGB(0x8, 1); SGB(0x100, 1); SGB(0x8, 1); SGB(0x100, 1); SGB(0x8, 1); SGB(0x100, 1); SGB(0x8, 1); SGB(0x100, 1); \
                   SGB(0x8, 1); SGB(0x100, 2); SGB(0x8, 1); SGB(0x100, 2); SGB(0x8, 3); } while (0)
#define MA(i) mf(acc[(i) / 4][(i) % 4], c.tr[(i) / 4], c.nat[(i) % 4])
#define LPU(reg, u, off) (smem + (reg)[sl >> 1][u] + (sl == 1 ? SLOT : 0) + (off))
#define LD_A(X0, X1, X2, X3, X4, X5, X6, X7, X8, X9)                                                        \
        X0; set_lo(n.tr[0], trd(LPU(aq, 0, ks * 4096))); set_hi(n.tr[0], trd(LPU(aq, 0, ks * 4096 + 512))); \
        X1; n.nat[0] = lds_b128(LPU(an, ks, 0));                                                            \
        X2; n.nat[1] = lds_b128(LPU(an, ks, 2048));                                                         \
        X3; n.nat[2] = lds_b128(LPU(an, ks, 4096));                                                         \
        X4; n.nat[3] = lds_b128(LPU(an, ks, 6144));                                                         \
        X5; set_lo(n.tr[1], trd(LPU(aq, 1, ks * 4096))); set_hi(n.tr[1], trd(LPU(aq, 1, ks * 4096 + 512))); \
        X6; set_lo(n.tr[2], trd(LPU(aq, 2, ks * 4096))); set_hi(n.tr[2], trd(LPU(aq, 2, ks * 4096 + 512))); \
        X7; X8; X9;
#define LD_B(X0, X1, X2, X3, X4, X5, X6, X7, X8, X9)                                                        \
        X0; set_lo(n.tr[0], trd(LPU(ao, 0, ks * 4096))); set_hi(n.tr[0], trd(LPU(ao, 0, ks * 4096 + 512))); \
        X1; n.nat[0] = lds_b128(LPU(anA, ks, 0));                                                           \
            set_lo(n.tr[2], trd(LPU(ao, 2, ks * 2048)));                                                    \
        X2; n.nat[1] = lds_b128(LPU(anA, ks, 2048));                                                        \
            set_hi(n.tr[2], trd(LPU(ao, 2, ks * 2048 + 256)));                                              \
        X3; n.nat[2] = lds_b128(LPU(anB, ks, 0));                                                           \
        X4; n.nat[3] = lds_b128(LPU(anB, ks, 2048));                                                        \
        X5; set_lo(n.tr[1], trd(LPU(ao, 1, ks * 4096))); set_hi(n.tr[1], trd(LPU(ao, 1, ks * 4096 + 512))); \
        X6; X7; X8; X9;
    auto grp_AA = [&](Frags& n, const Frags& c, const int sl, const int ks) {
        SB();
        LD_A(MA(0), MA(1), MA(2), MA(3), MA(4), MA(5), MA(6), MA(7), MA(8), MA(9))
        MA(10); MA(11);
        ILV(); SGB(0x8, 2);
        SB();
    };
    auto grp_BA = [&](Frags& n, const Frags& c, const int sl, const int ks) {
        SB();
        LD_B(MA(0), MA(1), MA(2), MA(3), MA(4), MA(5), MA(6), MA(7), MA(8), MA(9))
        MA(10); MA(11);
        ILV(); SGB(0x8, 2);
        SB();
    };
#define MB0 mfz<Z>(acc[0][0], c.nat[0], c.tr[0])
#define MB1 mfz<Z>(acc[0][1], c.nat[1], c.tr[0])
#define MB2 mfz<Z>(acc[2][0], c.nat[0], c.tr[2])
#define MB3 mfz<Z>(acc[2][1], c.nat[1], c.tr[2])
#define MB4 mfz<Z>(acc[0][2], c.nat[2], c.tr[0])
#define MB5 mfz<Z>(acc[0][3], c.nat[3], c.tr[0])
#define MB6 mfz<Z>(acc[1][0], c.nat[0], c.tr[1])
#define MB7 mfz<Z>(acc[1][1], c.nat[1], c.tr[1])
#define MB8 mfz<Z>(acc[1][2], c.nat[2], c.tr[1])
#define MB9 mfz<Z>(acc[1][3], c.nat[3], c.tr[1])
    auto grp_BB = [&](auto ZZ, Frags& n, const Frags& c, const int sl, const int ks) {
        constexpr bool Z = decltype(ZZ)::value;
        SB();
        LD_B(MB0, MB1, MB2, MB3, MB4, MB5, MB6, MB7, MB8, MB9)
        ILV();
        SB();
    };
    auto mma_B = [&](const Frags& c) { constexpr bool Z = false; MB0; MB1; MB2; MB3; MB4; MB5; MB6; MB7; MB8; MB9; };
    using TT = std::true_type; using FF = std::false_type;

    f32x4 oacc[3][4];
#pragma unroll
    for (int u = 0; u < 3; ++u)
#pragma unroll
        for (int t = 0; t < 4; ++t) { oacc[u][t] = f32x4{0.f, 0.f, 0.f, 0.f}; asm volatile("" : "+v"(oacc[u][t])); }
    Frags f0, f1;
#pragma unroll
    for (int t = 0; t < 4; ++t) f1.nat[t] = u32x4{0, 0, 0, 0};
#pragma unroll
    for (int u = 0; u < 3; ++u) f1.tr[u] = u32x4{0, 0, 0, 0};
    static_for<0, 10>([&](auto T_) {
        constexpr int t = decltype(T_)::value;
        if constexpr (t == 0) wait_vm<4>();
        if constexpr (t == 1) wait_vm<0>();
        bar();
        grp_AA(f0, f1, t % 3, 0);
        grp_AA(f1, f0, t % 3, 1);
        wait_lgkm0();
    });
    STAMP(1);
    u32x4 vw[4][5];
    auto cvt_vw = [&](u32x4 (&v)[5]) {
#pragma unroll
        for (int u = 0; u < 2; ++u) {
            v[2 * u] = pack8(acc[u][0], acc[u][1]);
            v[2 * u + 1] = pack8(acc[u][2], acc[u][3]);
        }
        v[4] = pack8(acc[2][0], acc[2][1]);
    };
    auto load_P = [&](u32x4 (&pf)[4], int m, int p) {
        const int d = (b + m) & 3;
        LAS char* pb = smem + aP[p] + (d == 0 ? L_POWN - L_K : (d - 1) * 8192);
#pragma unroll
        for (int t = 0; t < 4; ++t) pf[t] = lds_b128(pb + t * 2048);
    };
    auto mma_C = [&](const u32x4 (&v)[5], const u32x4 (&pf)[4], int p) {
#pragma unroll
        for (int t = 0; t < 4; ++t) {
            mf(oacc[0][t], v[p], pf[t]);
            mf(oacc[1][t], v[2 + p], pf[t]);
            if (p == 0) mf(oacc[2][t], v[4], pf[t]);
        }
    };
    {
        constexpr int sl = 4;
        bar();
        grp_BA(f0, f1, sl, 0);
        u32x2 qv[3][4];
#pragma unroll
        for (int u = 0; u < 3; ++u)
#pragma unroll
            for (int t = 0; t < 4; ++t) qv[u][t] = u32x2{pk2(acc[u][t][0], acc[u][t][1]), pk2(acc[u][t][2], acc[u][t][3])};
        {
            constexpr bool Z = true;
            Frags& n = f1; const Frags& c = f0; const int ks = 1;
            LD_B(MB0, MB1, MB2, MB3, MB4, MB5, MB6, MB7, MB8, MB9)
        }
#pragma unroll
        for (int u = 0; u < 3; ++u) {
            LAS char* qb = smem + aW[u];
#pragma unroll
            for (int t = 0; t < 4; ++t) *(LAS u32x2*)(qb + t * 2048) = qv[u][t];
        }
        SGB(0x2, 24);
#pragma unroll
        for (int i = 0; i < 10; ++i) { SGB(0x8, 1); SGB(0x100, 1); SGB(0x200, 1); }
        SGB(0x200, 2);
        SB();
        wait_lgkm0();
    }
    STAMP(2);
    {
        constexpr int sl = b_region(11);
        bar();
        SB();
        f32x4 s[4];
        u32x4 kf[2][4], qf[2];
        auto ld_kq = [&](int k6) {
            const int o = (k6 >> 1) * 8192;
            qf[k6 & 1] = lds_b128(smem + aQ[k6 & 1] + o);
#pragma unroll
            for (int t = 0; t < 4; ++t) kf[k6 & 1][t] = lds_b128(smem + aK[k6 & 1] + o + t * 2048);
        };
        ld_kq(0);
        ld_kq(1);
        {
            constexpr bool Z = false; const Frags& c = f1;
            MB0; MB1; MB2; MB3; MB4; MB5; MB6; MB7; MB8; MB9;
        }
        SB();
#pragma unroll
        for (int k6 = 0; k6 < 6; ++k6) {
#pragma unroll
            for (int t = 0; t < 4; ++t) s[t] = mfma(kf[k6 & 1][t], qf[k6 & 1], k6 == 0 ? f32x4{0.f, 0.f, 0.f, 0.f} : s[t]);
            SB();
            if (k6 + 2 < 6) ld_kq(k6 + 2);
            else if (k6 == 4) { Frags& n = f0; const int ks = 0; LD_B(, , , , , , , , , ) }
            SB();
        }
        float mx = fmaxf(fmaxf(fmaxf(s[0][0], s[0][1]), fmaxf(s[0][2], s[0][3])), fmaxf(fmaxf(s[1][0], s[1][1]), fmaxf(s[1][2], s[1][3])));
        mx = fmaxf(mx, fmaxf(fmaxf(fmaxf(s[2][0], s[2][1]), fmaxf(s[2][2], s[2][3])), fmaxf(fmaxf(s[3][0], s[3][1]), fmaxf(s[3][2], s[3][3]))));
        mx = xmax(mx);
        constexpr float C = 0.07216878364870322f * 1.4426950408889634f;
        const float mc = mx * C;
#pragma unroll
        for (int t = 0; t < 4; ++t)
#pragma unroll
            for (int i = 0; i < 4; ++i) s[t][i] = __builtin_amdgcn_exp2f(fmaf(s[t][i], C, -mc));
        const f32x4 s01 = s[0] + s[1], s23 = s[2] + s[3], s4 = s01 + s23;
        const float rs = __builtin_amdgcn_rcpf(xsum((s4[0] + s4[1]) + (s4[2] + s4[3])));
        const u32x4 pf0 = not_empty(pack8(s[0] * rs, s[1] * rs)), pf1 = not_empty(pack8(s[2] * rs, s[3] * rs));
        {
            constexpr bool Z = false; Frags& n = f1; const Frags& c = f0; const int ks = 1;
            LD_B(MB0, MB1, MB2, MB3, MB4, MB5, MB6, MB7, MB8, MB9)
        }
        __builtin_amdgcn_raw_buffer_store_b128(pf0, rws, WS_X + bid * XCH + w * 2048 + lane * 16, 0, AUX_SC1);
        __builtin_amdgcn_raw_buffer_store_b128(pf1, rws, WS_X + bid * XCH + w * 2048 + 1024 + lane * 16, 0, AUX_SC1);
        *(LAS u32x4*)(smem + aPW) = pf0;
        *(LAS u32x4*)(smem + aPW + 1024) = pf1;
#pragma unroll
        for (int i = 0; i < 10; ++i) { SGB(0x8, 1); SGB(0x100, 1); SGB(0x2, 9); }
        SB();
        wait_lgkm0();
    }
    auto stage = [&](auto S_) {
        constexpr int s = decltype(S_)::value;
        constexpr int c3 = s % 3;
        constexpr int sl = b_region(10 + s);
        bar();
        grp_BB(FF{}, f0, f1, sl, 0);
        if constexpr (c3 == 0) cvt_vw(vw[s / 3 - 1]);
        u32x4 pf[4], pg[4];
        {
            constexpr bool Z = c3 == 0; Frags& n = f1; const Frags& c = f0; const int ks = 1;
            SB();
            if constexpr (s == 8 || s == 9) { load_P(pf, s - 8, 0); load_P(pg, s - 8, 1); }
            if constexpr (s == 10 || s == 11) load_P(pf, 2, s - 10);
            LD_B(MB0, MB1, MB2, MB3, MB4, MB5, MB6, MB7, MB8, MB9)
            if constexpr (s == 8 || s == 9) { mma_C(vw[s - 8], pf, 0); mma_C(vw[s - 8], pg, 1); }
            if constexpr (s == 10 || s == 11) mma_C(vw[2], pf, s - 10);
            if constexpr (s == 8 || s == 9) {
#pragma unroll
                for (int i = 0; i < 9; ++i) { SGB(0x8, 1); SGB(0x100, 2); }
                SGB(0x8, 21);
            } else if constexpr (s >= 10) {
#pragma unroll
                for (int i = 0; i < 6; ++i) { SGB(0x8, 1); SGB(0x100, 2); }
                SGB(0x8, 1); SGB(0x100, 1); SGB(0x8, 1); SGB(0x100, 1);
                SGB(0x8, 14);
            } else ILV();
            SB();
        }
        wait_lgkm0();
    };
    static_for<2, 12>(stage);
    STAMP(3);
    mma_B(f1);
    {
        u32x4 pa[4], pb[4];
        load_P(pa, 3, 0);
        load_P(pb, 3, 1);
        cvt_vw(vw[3]);
        mma_C(vw[3], pa, 0);
        mma_C(vw[3], pb, 1);
    }
    STAMP(4);

    {
        const unsigned ob = WS_X + ((bid & ~7) | (l15 >> 2)) * XCH + 8192 + g * INB + (lg * 4 + (l15 & 3)) * 16;
#pragma unroll
        for (int u = 0; u < 3; ++u)
#pragma unroll
            for (int m = 0; m < 2; ++m)
                __builtin_amdgcn_raw_buffer_store_b128(pack8(oacc[u][2 * m], oacc[u][2 * m + 1]), rws,
                                                       ob + m * (4 * XCH) + (u < 2 ? 2 * w + u : 8 + w) * 256, 0, AUX_SC1);
        STAMP(5);
        wait_vm<0>();
        STAMP(6);
        if (lane == 0) __hip_atomic_store((gu32*)(ws + WS_OF + bid * 128 + w * 4), epoch, RLX_AGENT);
        if (w == 0) {
            const unsigned fo = WS_OF + ((bid & ~7) | (lane & 7)) * 128;
#pragma unroll 1
            for (unsigned spin = 0; spin < SPIN_CAP; ++spin) {
                asm volatile("" ::: "memory");
                const u32x4 f = __builtin_amdgcn_raw_buffer_load_b128(rws, fo, 0, AUX_SC1);
                if (__all(((f[0] ^ epoch) | (f[1] ^ epoch) | (f[2] ^ epoch) | (f[3] ^ epoch)) == 0)) break;
                __builtin_amdgcn_s_sleep(1);
            }
            __builtin_amdgcn_fence(__ATOMIC_ACQUIRE, "wavefront");
        }
        STAMP(7);
        bar();
        if (w < 3) {
            const int T = w * 64 + lane;
            u32x4 v[8];
#pragma unroll
            for (int s = 0; s < 8; ++s)
                v[s] = __builtin_amdgcn_raw_buffer_load_b128(rws, WS_X + bid * XCH + 8192 + s * INB + T * 16, 0, AUX_SC1);
            float r[8];
#pragma unroll
            for (int i = 0; i < 4; ++i) { r[2 * i] = bflo(v[0][i]); r[2 * i + 1] = bfhi(v[0][i]); }
#pragma unroll
            for (int s = 1; s < 8; ++s)
#pragma unroll
                for (int i = 0; i < 4; ++i) { r[2 * i] += bflo(v[s][i]); r[2 * i + 1] += bfhi(v[s][i]); }
            if (w == 2) {
#pragma unroll
                for (int i = 0; i < 8; ++i) r[i] = xsum32(r[i]);
            }
            if (T < 160) {
                const int U = T >> 4, gg = (T >> 2) & 3, qq = T & 3;
                const int row = 32 * (g >> 2) + 4 * (g & 3) + qq;
                const int col = (j & 1) ? (U < 8 ? 32 + 16 * U : 16 * (U - 8)) : 16 * U;
                u16* o = out + (b * SQ + row) * DM + j * 160 + col + 4 * gg;
                *(u32x2*)o = u32x2{pk2(r[0], r[1]), pk2(r[2], r[3])};
                *(u32x2*)(o + 16 * DM) = u32x2{pk2(r[4], r[5]), pk2(r[6], r[7])};
            }
        }
        STAMP(8);
        STAMP(9);
        STAMP_OUT();
        if (g == 0 && j == 0 && threadIdx.x == 0) __hip_atomic_store((gu32*)(ws + WS_E + b * 128), epoch, RLX_AGENT);
    }
}

extern "C" void kernel_launch(void* const* d_in, const int* in_sizes, int n_in, void* d_out, int out_size,
                              void* d_ws, size_t ws_size, hipStream_t stream) {
    mha_fused<<<256, 512, 0, stream>>>((const u16*)d_in[0], (const u16*)d_in[1], (const u16*)d_in[2],
                                       (const u16*)d_in[3], (const u16*)d_in[4], (u16*)d_out, (char*)d_ws);
}
```

```cpp
#include <hip/hip_runtime.h>
#include <stdint.h>
#include <type_traits>

#define LAS __attribute__((address_space(3)))
#define GAS __attribute__((address_space(1)))
typedef unsigned short u16;
typedef short s16x4 __attribute__((ext_vector_type(4)));
typedef short s16x8 __attribute__((ext_vector_type(8)));
typedef float f32x4 __attribute__((ext_vector_type(4)));
typedef float f32x2 __attribute__((ext_vector_type(2)));
typedef __bf16 bf16x2 __attribute__((ext_vector_type(2)));
typedef unsigned u32x4 __attribute__((ext_vector_type(4)));
typedef unsigned u32x2 __attribute__((ext_vector_type(2)));
typedef GAS unsigned gu32;
#define RLX_AGENT __ATOMIC_RELAXED, __HIP_MEMORY_SCOPE_AGENT
#define DI __device__ __forceinline__

namespace {

constexpr int NB = 8, SQ = 64, DM = 640, NH = 32, SK = 64, DH = 192, HD = NH * DH;
constexpr int AUX_SC1 = 16;

constexpr int SLOT = 32768;
constexpr int L_K = 3 * SLOT;
constexpr int L_I10 = L_K + 24576;
constexpr int L_Q = 1 * SLOT;
constexpr int L_POWN = L_I10 + 28672;
constexpr int LDS_BYTES = 163840;

constexpr unsigned WS_E = 0;
constexpr unsigned WS_OF = 4096 + 256 * 128;
constexpr unsigned WS_RDY = 4096;
constexpr unsigned WS_X = 131072;
constexpr unsigned INB = 3072;
constexpr unsigned XCH = 8192 + 8 * INB;
constexpr unsigned WS_END = WS_X + 256 * XCH;
constexpr unsigned SPIN_CAP = 1u << 16;
#define TAIL_SLEEP 8

#define STAMP(i)
#define STAMP_DECL
#define STAMP_OUT()
#define TR_DECL
#define TR()
#define TR_OUT()

template <int N> DI void wait_vm() { asm volatile("s_waitcnt vmcnt(%0)" ::"i"(N) : "memory"); }
DI void bar() { asm volatile("s_barrier" ::: "memory"); }
#define HTR_DECL
#define HTR()
#define HTR_OUT()
#define SB() __builtin_amdgcn_sched_barrier(0)
#define SGB(mask, n) __builtin_amdgcn_sched_group_barrier(mask, n, 0)
DI void wait_lgkm0() { __builtin_amdgcn_s_waitcnt(0xC07F); asm volatile("" ::: "memory"); }

DI f32x4 mfma(u32x4 a, u32x4 b, f32x4 c) {
    return __builtin_amdgcn_mfma_f32_16x16x32_bf16(__builtin_bit_cast(s16x8, a), __builtin_bit_cast(s16x8, b), c, 0, 0, 0);
}
DI void mf(f32x4& c, u32x4 a, u32x4 b) { c = mfma(a, b, c); }
template <bool Z> DI void mfz(f32x4& c, u32x4 a, u32x4 b) { c = mfma(a, b, Z ? f32x4{0.f, 0.f, 0.f, 0.f} : c); }
DI int opq(int v) { asm volatile("" : "+v"(v)); return v; }
DI u32x2 trd(LAS char* p) { return __builtin_bit_cast(u32x2, __builtin_amdgcn_ds_read_tr16_b64_v4i16((LAS s16x4*)p)); }
DI void set_lo(u32x4& v, u32x2 x) { v[0] = x[0]; v[1] = x[1]; }
DI void set_hi(u32x4& v, u32x2 x) { v[2] = x[0]; v[3] = x[1]; }
DI u32x4 lds_b128(LAS char* p) { return *(LAS u32x4*)p; }
DI u32x4 lds_tr(LAS char* p, int hi_off) {
    u32x2 lo = __builtin_bit_cast(u32x2, __builtin_amdgcn_ds_read_tr16_b64_v4i16((LAS s16x4*)p));
    u32x2 hi = __builtin_bit_cast(u32x2, __builtin_amdgcn_ds_read_tr16_b64_v4i16((LAS s16x4*)(p + hi_off)));
    return u32x4{lo[0], lo[1], hi[0], hi[1]};
}
DI unsigned pk2(float a, float b) {
    f32x2 v = {a, b};
    return __builtin_bit_cast(unsigned, __builtin_convertvector(v, bf16x2));
}
DI u32x4 pack8(f32x4 a, f32x4 b) { return u32x4{pk2(a[0], a[1]), pk2(a[2], a[3]), pk2(b[0], b[1]), pk2(b[2], b[3])}; }
DI float bflo(unsigned w) { return __uint_as_float(w << 16); }
DI float bfhi(unsigned w) { return __uint_as_float(w & 0xffff0000u); }
DI float xmax(float v) {
    auto a = __builtin_amdgcn_permlane16_swap(__float_as_uint(v), __float_as_uint(v), false, false);
    v = fmaxf(__uint_as_float(a[0]), __uint_as_float(a[1]));
    auto c = __builtin_amdgcn_permlane32_swap(__float_as_uint(v), __float_as_uint(v), false, false);
    return fmaxf(__uint_as_float(c[0]), __uint_as_float(c[1]));
}
DI float xsum(float v) {
    auto a = __builtin_amdgcn_permlane16_swap(__float_as_uint(v), __float_as_uint(v), false, false);
    v = __uint_as_float(a[0]) + __uint_as_float(a[1]);
    auto c = __builtin_amdgcn_permlane32_swap(__float_as_uint(v), __float_as_uint(v), false, false);
    return __uint_as_float(c[0]) + __uint_as_float(c[1]);
}
DI float xsum32(float v) {
    auto c = __builtin_amdgcn_permlane32_swap(__float_as_uint(v), __float_as_uint(v), false, false);
    return __uint_as_float(c[0]) + __uint_as_float(c[1]);
}

struct Frags { u32x4 nat[4]; u32x4 tr[3]; };
template <int I, int N, class F> DI void static_for(F&& f) {
    if constexpr (I < N) { f(std::integral_constant<int, I>{}); static_for<I + 1, N>(f); }
}

DI unsigned sload(const char* p) { unsigned v; asm volatile("s_load_dword %0, %1, 0x0\n\ts_waitcnt lgkmcnt(0)" : "=s"(v) : "s"(p) : "memory"); return v; }
DI void gld(u32x4& d, __amdgpu_buffer_rsrc_t r, unsigned voff, unsigned soff) {
    asm volatile("buffer_load_dwordx4 %0, %1, %2, %3 offen" : "=v"(d) : "v"(voff), "s"(r), "s"(soff) : "memory");
}
DI void gld_sc1(u32x4& d, __amdgpu_buffer_rsrc_t r, unsigned voff) {
    asm volatile("buffer_load_dwordx4 %0, %1, %2, 0 offen sc1" : "=v"(d) : "v"(voff), "s"(r) : "memory");
}
DI void dma1(__amdgpu_buffer_rsrc_t r, unsigned lds_dst, unsigned voff, unsigned soff) {
    asm volatile("s_mov_b32 m0, %0\n\ts_nop 0\n\tbuffer_load_dwordx4 %1, %2, %3 offen lds" :: "s"(lds_dst), "v"(voff), "s"(r), "s"(soff) : "memory");
}
template <int N> DI void wait_vm_for(u32x4& a) { asm volatile("s_waitcnt vmcnt(%1)" : "+v"(a) : "i"(N) : "memory"); }
DI void landed(u32x4& a) { asm volatile("" : "+v"(a) :: "memory"); }

#define PF_LO 2
#define PF_SPAN 6
constexpr int PF_N = 52;
#define PF_B1 28
static_assert(PF_LO + PF_SPAN - 1 <= 8 && PF_B1 >= 28, "");
#define P_ISSUE 15
constexpr int b_region(int it) { return it == 10 ? 4 : (it == 11 ? 2 : (it == 12 ? 0 : ((it - 13) % 4 == 0 ? 4 : ((it - 13) % 4 == 1 ? 1 : ((it - 13) % 4 == 2 ? 2 : 0))))); }
constexpr int region_base(int r) { return r == 4 ? L_I10 : r * SLOT; }

constexpr unsigned EMPTY = 0xffffffffu;
DI u32x4 not_empty(u32x4 v) { return u32x4{min(v[0], EMPTY - 1), min(v[1], EMPTY - 1), min(v[2], EMPTY - 1), min(v[3], EMPTY - 1)}; }
DI unsigned umax4(u32x4 v) { return max(max(v[0], v[1]), max(v[2], v[3])); }

}

__global__ __launch_bounds__(512, 2) void mha_fused(const u16* __restrict__ x, const u16* __restrict__ kc,
                                                    const u16* __restrict__ vc, const u16* __restrict__ wq,
                                                    const u16* __restrict__ wo, u16* __restrict__ out,
                                                    char* __restrict__ ws) {
    __shared__ __attribute__((aligned(16))) char smem_[LDS_BYTES];
    LAS char* smem = (LAS char*)smem_;
    const int bid = blockIdx.x;
    const int g = bid & 7, j = (bid >> 3) & 3, b = bid >> 5;
    const int h = 4 * g + j;
    const int wave = __builtin_amdgcn_readfirstlane(threadIdx.x >> 6);
    const int lane = threadIdx.x & 63;
    int ksA0 = ((b * 10) >> 3) + ((j * 5) >> 1); if (ksA0 >= 10) ksA0 -= 10;
    const int hrot = (j + b) & 3;
    const int crot = b >> 2;
    STAMP_DECL
    STAMP(0);
    const int r8 = lane >> 3, c8 = lane & 7;
    const unsigned sw8 = (unsigned)((c8 ^ r8) * 16);
    const unsigned swt = (unsigned)((2 * ((c8 >> 1) ^ ((r8 >> 1) & 1)) + (c8 & 1)) * 16);
    const unsigned vx = (unsigned)(r8 * (DM * 2)) + sw8;
    const __amdgpu_buffer_rsrc_t rx = __builtin_amdgcn_make_buffer_rsrc((void*)x, 0, NB * SQ * DM * 2, 0x00020000);
    const __amdgpu_buffer_rsrc_t rq = __builtin_amdgcn_make_buffer_rsrc((void*)wq, 0, DM * HD * 2, 0x00020000);
#define DMA(rs, dst, voff, soff) __builtin_amdgcn_raw_ptr_buffer_load_lds(rs, (LAS void*)(dst), 16, voff, soff, 0, 0)
    {
        const unsigned vq = (unsigned)(r8 * (HD * 2)) + (swt ^ (unsigned)((wave & 1) * 64));
        const unsigned sx = (unsigned)((b * 64 + 8 * wave) * (DM * 2)), so = (unsigned)(8 * wave * (HD * 2) + h * (DH * 2));
#pragma unroll
        for (int it = 0; it < 2; ++it) {
            LAS char* sb = smem + it * SLOT + wave * 1024;
            const int ks = ksA0 + it == 10 ? 0 : ksA0 + it;
#pragma unroll
            for (int blk = 0; blk < 3; ++blk) DMA(rq, sb + 8192 + blk * 8192, vq, so + ks * 64 * (HD * 2) + blk * 128);
            DMA(rx, sb, vx, sx + ks * 128);
        }
    }
    STAMP(3);

    if (wave >= 4) {
        TR_DECL
#define BARL() do { bar(); } while (0)
#define BARH() do { bar(); HTR(); } while (0)
        const __amdgpu_buffer_rsrc_t rv = __builtin_amdgcn_make_buffer_rsrc((void*)vc, 0, NB * NH * SK * DH * 2, 0x00020000);
        const __amdgpu_buffer_rsrc_t ro = __builtin_amdgcn_make_buffer_rsrc((void*)wo, 0, HD * DM * 2, 0x00020000);
        const unsigned vkv = (unsigned)(r8 * (DH * 2)) + sw8;
        const unsigned vh = (unsigned)((lane >> 2) * (DM * 2) + ((lane & 3) ^ ((lane >> 4) & 2)) * 16);
        const unsigned colF = (unsigned)(j * 320 + (j & 1) * 64), colH = (unsigned)(j * 320 + ((j & 1) ? 0 : 256));
        auto stage_hh = [&](int s) { return 4 * g + ((hrot + s / 3) & 3); };
        auto stage_ch = [&](int s) { int c = s % 3 + crot; return c >= 3 ? c - 3 : c; };
        const __amdgpu_buffer_rsrc_t rws = __builtin_amdgcn_make_buffer_rsrc((void*)ws, 0, WS_END, 0x00020000);
        if (wave < 7) {
            const int lw = wave - 4;
            const unsigned swA = swt ^ (unsigned)((lw & 1) * 64), swB = swA ^ 64u;
            const unsigned vqA = (unsigned)(r8 * (HD * 2)) + swA, vqB = (unsigned)(r8 * (HD * 2)) + swB;
            const unsigned voA = (unsigned)(r8 * (DM * 2)) + swA, voB = (unsigned)(r8 * (DM * 2)) + swB;
            LAS char* const sw = smem + lw * 1024;
            const unsigned sx0 = (unsigned)((b * 64 + 8 * lw) * (DM * 2));
            const unsigned sq0 = (unsigned)(8 * lw * (HD * 2) + h * (DH * 2));
            auto run = [&](auto THREE) {
                constexpr bool three = decltype(THREE)::value;
                constexpr int NA = three ? 12 : 8, NBI = three ? 9 : 10, NP = three ? 3 : 2;
                wait_vm<4>();
                static_for<2, 10>([&](auto IT) {
                    constexpr int it = decltype(IT)::value;
                    int ks = ksA0 + it; if (ks >= 10) ks -= 10;
                    const unsigned sx = sx0 + ks * 128, so = sq0 + ks * 64 * (HD * 2);
                    LAS char* sb = sw + (it % 3) * SLOT;
                    SB();
                    if constexpr (it > 2) wait_vm<NA>();
                    BARL();
                    SB();
#pragma unroll
                    for (int rg = 0; rg < (three ? 3 : 2); ++rg) {
                        DMA(rx, sb + rg * 3072, vx, sx + rg * 24 * DM * 2);
#pragma unroll
                        for (int blk = 0; blk < 3; ++blk)
                            DMA(rq, sb + 8192 + blk * 8192 + rg * 3072, rg == 1 ? vqB : vqA, so + blk * 128 + rg * 24 * HD * 2);
                    }
                });
                wait_vm<NA>(); BARL();
                static_for<11, 22>([&](auto IT) {
                    constexpr int it = decltype(IT)::value;
                    const int hh = stage_hh(it - 10), ch = stage_ch(it - 10);
                    const unsigned sv = (unsigned)(((b * NH + hh) * SK + 8 * lw) * (DH * 2) + ch * 128);
                    const unsigned so = (unsigned)((hh * DH + ch * 64) * (DM * 2));
                    LAS char* sb = sw + region_base(b_region(it));
                    SB();
                    if constexpr (it == 11) wait_vm<0>();
                    else if constexpr (it > 12) {
                        if constexpr (it - 1 >= 17 && it - 1 < 21) { if (((b + it - 18) & 3) != 0) wait_vm<NBI + NP>(); else wait_vm<NBI>(); }
                        else wait_vm<NBI>();
                    }
                    BARL();
                    SB();
#pragma unroll
                    for (int rg = 0; rg < (three ? 3 : 2); ++rg) {
                        DMA(rv, sb + rg * 3072, vkv, sv + rg * 24 * DH * 2);
#pragma unroll
                        for (int blk = 0; blk < 2; ++blk)
                            DMA(ro, sb + 8192 + blk * 8192 + rg * 3072, rg == 1 ? voB : voA, so + colF + blk * 128 + (8 * lw + rg * 24) * DM * 2);
                    }
                    if constexpr (!three) {
#pragma unroll
                        for (int q = 0; q < 4; ++q) DMA(ro, sb - lw * 1024 + 24576 + q * 1024, vh, so + colH + q * 16 * DM * 2);
                    }
                    if constexpr (it >= 17 && it < 21) {
                        const int d = (b + it - 17) & 3;
                        if (d != 0) {
                            const unsigned src = WS_X + (unsigned)(((bid & ~24) | (((j + d) & 3) << 3)) * XCH);
#pragma unroll
                            for (int i = 0; i < NP; ++i)
                                __builtin_amdgcn_raw_ptr_buffer_load_lds(rws, (LAS void*)(smem + L_K + (d - 1) * 8192 + (lw + 3 * i) * 1024), 16,
                                                                         (unsigned)(lane * 16), src + (lw + 3 * i) * 1024, 0, AUX_SC1);
                        }
                    }
                });
                wait_vm<NBI>(); BARL();
                wait_vm<0>(); BARL();
            };
            if (lw < 2) run(std::true_type{}); else run(std::false_type{});
            STAMP(2);
        } else {
            HTR_DECL
            const __amdgpu_buffer_rsrc_t rk = __builtin_amdgcn_make_buffer_rsrc((void*)kc, 0, NB * NH * SK * DH * 2, 0x00020000);
            const unsigned vo0 = (unsigned)(r8 * (DM * 2)) + swt, vo1 = (unsigned)(r8 * (DM * 2)) + (swt ^ 64u);
            auto piece_load = [&](auto N_) {
                constexpr int n = decltype(N_)::value;
                if constexpr (n < 28) {
                    const int hh = stage_hh(0), ch = stage_ch(0);
                    const unsigned so = (unsigned)((hh * DH + ch * 64) * (DM * 2));
                    constexpr unsigned dst = L_I10 + n * 1024;
                    if constexpr (n < 8) dma1(rv, dst, vkv, (unsigned)(((b * NH + hh) * SK + n * 8) * (DH * 2) + ch * 128));
                    else if constexpr (n < 24) dma1(ro, dst, (n & 1) ? vo1 : vo0, so + colF + ((n - 8) >> 3) * 128 + (n & 7) * 8 * (DM * 2));
                    else dma1(ro, dst, vh, so + colH + (n - 24) * 16 * (DM * 2));
                } else {
                    constexpr int st = (n - 28) / 8, rg = (n - 28) % 8;
                    dma1(rk, (unsigned)(L_K + st * 8192 + rg * 1024), vkv, (unsigned)(((b * NH + h) * SK + rg * 8) * (DH * 2) + st * 128));
                }
            };
            const unsigned epoch = sload(ws + WS_E + b * 128) + 1u;
            HTR();
            wait_vm<4>();
            const int tb = PF_LO + (bid >> 3) % PF_SPAN;
            const u32x4 e4 = {EMPTY, EMPTY, EMPTY, EMPTY};
            BARH();
            wait_vm<0>();
            BARH();
#pragma unroll 1
            for (int t = 2; t < 9; ++t) {
                BARH();
                if (t == tb) {
                    static_for<0, PF_B1>(piece_load);
#pragma unroll
                    for (int i = 0; i < 8; ++i)
                        __builtin_amdgcn_raw_buffer_store_b128(e4, rws, WS_X + bid * XCH + i * 1024 + lane * 16, 0, AUX_SC1);
                }
            }
            BARH();
            static_for<PF_B1, 52>(piece_load);
            wait_vm<52 - PF_B1>();
            if (lane == 0) __hip_atomic_store((gu32*)(ws + WS_RDY + bid * 128), epoch, RLX_AGENT);
            const int peer = (bid & ~24) | (((j + 1 + (lane & 3)) & 3) << 3);
            unsigned rdy;
            const int aPH = opq(L_K + lane * 16);
            static_for<10, 22>([&](auto T_) {
                constexpr int t = decltype(T_)::value;
                BARH();
                if constexpr (t == 10) wait_vm<0>();
                if constexpr (t == 13) {
                    asm volatile("buffer_load_dword %0, %1, %2, 0 offen sc1" : "=v"(rdy) : "v"(WS_RDY + peer * 128), "s"(rws) : "memory");
                }
                if constexpr (t == 14) {
                    asm volatile("s_waitcnt vmcnt(0)" : "+v"(rdy) :: "memory");
#pragma unroll 1
                    for (unsigned spin = 0; spin < SPIN_CAP && !__all(rdy == epoch); ++spin) {
                        __builtin_amdgcn_s_sleep(1);
                        asm volatile("buffer_load_dword %0, %1, %2, 0 offen sc1\n\ts_waitcnt vmcnt(0)" : "=&v"(rdy) : "v"(WS_RDY + peer * 128), "s"(rws) : "memory");
                    }
                }
                if constexpr (t >= 17 && t < 21) {
                    constexpr int m = t - 17;
                    const int d = (b + m) & 3;
                    if (d != 0) {
                        unsigned mx = 0;
#pragma unroll
                        for (int i = 0; i < 8; ++i) mx = max(mx, umax4(*(LAS u32x4*)(smem + aPH + (d - 1) * 8192 + i * 1024)));
                        if (!__all(mx != EMPTY)) {
                            u32x4 p[8];
#pragma unroll 1
                            for (unsigned spin = 0; spin < SPIN_CAP; ++spin) {
#pragma unroll
                                for (int i = 0; i < 8; ++i)
                                    gld_sc1(p[i], rws, WS_X + ((bid & ~24) | (((j + d) & 3) << 3)) * XCH + i * 1024 + lane * 16);
                                wait_vm<0>();
                                mx = 0;
#pragma unroll
                                for (int i = 0; i < 8; ++i) { landed(p[i]); mx = max(mx, umax4(p[i])); }
                                if (__all(mx != EMPTY)) break;
                            }
#pragma unroll
                            for (int i = 0; i < 8; ++i) *(LAS u32x4*)(smem + aPH + (d - 1) * 8192 + i * 1024) = p[i];
                        }
                    }
                }
                wait_lgkm0();
            });
            HTR_OUT();
        }
        TR_OUT();
        STAMP_OUT();
        return;
    }

    const int w = wave;
    const int l15 = lane & 15, lg = lane >> 4;
    const __amdgpu_buffer_rsrc_t rws = __builtin_amdgcn_make_buffer_rsrc((void*)ws, 0, WS_END, 0x00020000);
    const unsigned epoch = sload(ws + WS_E + b * 128) + 1u;

    const int an0 = l15 * 128 + ((lg ^ (l15 & 7)) << 4);
    const int q4 = l15 >> 2, p4 = l15 & 3;
    const int sq = ((q4 >> 1) & 1) | ((lg & 1) << 1);
    const int kx = (w >> 1) * 4096;
    int aq[2][3], ao[3][3], an[2][2], anA[3][2], anB[3][2];
#pragma unroll
    for (int hi = 0; hi < 3; ++hi) {
        const int base = hi == 2 ? L_I10 : hi * 65536;
        if (hi < 2) {
#pragma unroll
            for (int u = 0; u < 3; ++u) {
                const int T = 3 * w + u;
                aq[hi][u] = opq(base + 8192 + (T >> 2) * 8192 + (8 * lg + q4) * 128 + (((T & 3) ^ sq) << 5) + p4 * 8);
            }
            an[hi][0] = opq(base + an0);
            an[hi][1] = opq(base + (an0 ^ 64));
        }
#pragma unroll
        for (int u = 0; u < 2; ++u) {
            const int T = 2 * w + u;
            ao[hi][u] = opq(base + 8192 + (T >> 2) * 8192 + (8 * lg + q4) * 128 + (((T & 3) ^ sq) << 5) + p4 * 8);
        }
        ao[hi][2] = opq(base + 24576 + (8 * lg + q4) * 64 + (((w & 1) ^ (lg & 1)) << 5) + p4 * 8);
#pragma unroll
        for (int ks = 0; ks < 2; ++ks) {
            anA[hi][ks] = opq(base + (an0 ^ (ks << 6)) + kx);
            anB[hi][ks] = opq(base + (an0 ^ (ks << 6)) + 4096 - kx);
        }
    }

    int aW[3];
#pragma unroll
    for (int u = 0; u < 3; ++u) {
        const int T = 3 * w + u;
        aW[u] = opq(L_Q + (T >> 2) * 8192 + l15 * 128 + (((2 * (T & 3) + (lg >> 1)) ^ (l15 & 7)) << 4) + (lg & 1) * 8);
    }
    const int aPW = opq(L_POWN + w * 2048 + lane * 16);
    const int aK[2] = {opq(L_K + an0), opq(L_K + (an0 ^ 64))};
    const int aQ[2] = {opq(L_Q + w * 2048 + an0), opq(L_Q + w * 2048 + (an0 ^ 64))};
    const int aP[2] = {opq(L_K + lane * 16 + (w >> 1) * 1024), opq(L_K + lane * 16 + 1024 - (w >> 1) * 1024)};
    f32x4 acc[3][4];
#pragma unroll
    for (int u = 0; u < 3; ++u)
#pragma unroll
        for (int t = 0; t < 4; ++t) { acc[u][t] = f32x4{0.f, 0.f, 0.f, 0.f}; asm volatile("" : "+v"(acc[u][t])); }
#define ILV() do { SGB(0x8, 1); SGB(0x100, 2); SGB(0x8, 1); SGB(0x100, 1); SGB(0x8, 1); SGB(0x100, 1); SGB(0x8, 1); SGB(0x100, 1); SGB(0x8, 1); SGB(0x100, 1); \
                   SGB(0x8, 1); SGB(0x100, 2); SGB(0x8, 1); SGB(0x100, 2); SGB(0x8, 3); } while (0)
#define MA(i) mf(acc[(i) / 4][(i) % 4], c.tr[(i) / 4], c.nat[(i) % 4])
#define LPU(reg, u, off) (smem + (reg)[sl >> 1][u] + (sl == 1 ? SLOT : 0) + (off))
#define LD_A(X0, X1, X2, X3, X4, X5, X6, X7, X8, X9)                                                        \
        X0; set_lo(n.tr[0], trd(LPU(aq, 0, ks * 4096))); set_hi(n.tr[0], trd(LPU(aq, 0, ks * 4096 + 512))); \
        X1; n.nat[0] = lds_b128(LPU(an, ks, 0));                                                            \
        X2; n.nat[1] = lds_b128(LPU(an, ks, 2048));                                                         \
        X3; n.nat[2] = lds_b128(LPU(an, ks, 4096));                                                         \
        X4; n.nat[3] = lds_b128(LPU(an, ks, 6144));                                                         \
        X5; set_lo(n.tr[1], trd(LPU(aq, 1, ks * 4096))); set_hi(n.tr[1], trd(LPU(aq, 1, ks * 4096 + 512))); \
        X6; set_lo(n.tr[2], trd(LPU(aq, 2, ks * 4096))); set_hi(n.tr[2], trd(LPU(aq, 2, ks * 4096 + 512))); \
        X7; X8; X9;
#define LD_B(X0, X1, X2, X3, X4, X5, X6, X7, X8, X9)                                                        \
        X0; set_lo(n.tr[0], trd(LPU(ao, 0, ks * 4096))); set_hi(n.tr[0], trd(LPU(ao, 0, ks * 4096 + 512))); \
        X1; n.nat[0] = lds_b128(LPU(anA, ks, 0));                                                           \
            set_lo(n.tr[2], trd(LPU(ao, 2, ks * 2048)));                                                    \
        X2; n.nat[1] = lds_b128(LPU(anA, ks, 2048));                                                        \
            set_hi(n.tr[2], trd(LPU(ao, 2, ks * 2048 + 256)));                                              \
        X3; n.nat[2] = lds_b128(LPU(anB, ks, 0));                                                           \
        X4; n.nat[3] = lds_b128(LPU(anB, ks, 2048));                                                        \
        X5; set_lo(n.tr[1], trd(LPU(ao, 1, ks * 4096))); set_hi(n.tr[1], trd(LPU(ao, 1, ks * 4096 + 512))); \
        X6; X7; X8; X9;
    auto grp_AA = [&](Frags& n, const Frags& c, const int sl, const int ks) {
        SB();
        LD_A(MA(0), MA(1), MA(2), MA(3), MA(4), MA(5), MA(6), MA(7), MA(8), MA(9))
        MA(10); MA(11);
        ILV(); SGB(0x8, 2);
        SB();
    };
    auto grp_BA = [&](Frags& n, const Frags& c, const int sl, const int ks) {
        SB();
        LD_B(MA(0), MA(1), MA(2), MA(3), MA(4), MA(5), MA(6), MA(7), MA(8), MA(9))
        MA(10); MA(11);
        ILV(); SGB(0x8, 2);
        SB();
    };
#define MB0 mfz<Z>(acc[0][0], c.nat[0], c.tr[0])
#define MB1 mfz<Z>(acc[0][1], c.nat[1], c.tr[0])
#define MB2 mfz<Z>(acc[2][0], c.nat[0], c.tr[2])
#define MB3 mfz<Z>(acc[2][1], c.nat[1], c.tr[2])
#define MB4 mfz<Z>(acc[0][2], c.nat[2], c.tr[0])
#define MB5 mfz<Z>(acc[0][3], c.nat[3], c.tr[0])
#define MB6 mfz<Z>(acc[1][0], c.nat[0], c.tr[1])
#define MB7 mfz<Z>(acc[1][1], c.nat[1], c.tr[1])
#define MB8 mfz<Z>(acc[1][2], c.nat[2], c.tr[1])
#define MB9 mfz<Z>(acc[1][3], c.nat[3], c.tr[1])
    auto grp_BB = [&](auto ZZ, Frags& n, const Frags& c, const int sl, const int ks) {
        constexpr bool Z = decltype(ZZ)::value;
        SB();
        LD_B(MB0, MB1, MB2, MB3, MB4, MB5, MB6, MB7, MB8, MB9)
        ILV();
        SB();
    };
    auto mma_B = [&](const Frags& c) { constexpr bool Z = false; MB0; MB1; MB2; MB3; MB4; MB5; MB6; MB7; MB8; MB9; };
    using TT = std::true_type; using FF = std::false_type;

    f32x4 oacc[3][4];
#pragma unroll
    for (int u = 0; u < 3; ++u)
#pragma unroll
        for (int t = 0; t < 4; ++t) { oacc[u][t] = f32x4{0.f, 0.f, 0.f, 0.f}; asm volatile("" : "+v"(oacc[u][t])); }
    Frags f0, f1;
#pragma unroll
    for (int t = 0; t < 4; ++t) f1.nat[t] = u32x4{0, 0, 0, 0};
#pragma unroll
    for (int u = 0; u < 3; ++u) f1.tr[u] = u32x4{0, 0, 0, 0};
    static_for<0, 10>([&](auto T_) {
        constexpr int t = decltype(T_)::value;
        if constexpr (t == 0) wait_vm<4>();
        if constexpr (t == 1) wait_vm<0>();
        bar();
        grp_AA(f0, f1, t % 3, 0);
        grp_AA(f1, f0, t % 3, 1);
        wait_lgkm0();
    });
    STAMP(1);
    u32x4 vw[4][5];
    auto cvt_vw = [&](u32x4 (&v)[5]) {
#pragma unroll
        for (int u = 0; u < 2; ++u) {
            v[2 * u] = pack8(acc[u][0], acc[u][1]);
            v[2 * u + 1] = pack8(acc[u][2], acc[u][3]);
        }
        v[4] = pack8(acc[2][0], acc[2][1]);
    };
    auto load_P = [&](u32x4 (&pf)[4], int m, int p) {
        const int d = (b + m) & 3;
        LAS char* pb = smem + aP[p] + (d == 0 ? L_POWN - L_K : (d - 1) * 8192);
#pragma unroll
        for (int t = 0; t < 4; ++t) pf[t] = lds_b128(pb + t * 2048);
    };
    auto mma_C = [&](const u32x4 (&v)[5], const u32x4 (&pf)[4], int p) {
#pragma unroll
        for (int t = 0; t < 4; ++t) {
            mf(oacc[0][t], v[p], pf[t]);
            mf(oacc[1][t], v[2 + p], pf[t]);
            if (p == 0) mf(oacc[2][t], v[4], pf[t]);
        }
    };
    {
        constexpr int sl = 4;
        bar();
        grp_BA(f0, f1, sl, 0);
        u32x2 qv[3][4];
#pragma unroll
        for (int u = 0; u < 3; ++u)
#pragma unroll
            for (int t = 0; t < 4; ++t) qv[u][t] = u32x2{pk2(acc[u][t][0], acc[u][t][1]), pk2(acc[u][t][2], acc[u][t][3])};
        {
            constexpr bool Z = true;
            Frags& n = f1; const Frags& c = f0; const int ks = 1;
            LD_B(MB0, MB1, MB2, MB3, MB4, MB5, MB6, MB7, MB8, MB9)
        }
#pragma unroll
        for (int u = 0; u < 3; ++u) {
            LAS char* qb = smem + aW[u];
#pragma unroll
            for (int t = 0; t < 4; ++t) *(LAS u32x2*)(qb + t * 2048) = qv[u][t];
        }
        SGB(0x2, 24);
#pragma unroll
        for (int i = 0; i < 10; ++i) { SGB(0x8, 1); SGB(0x100, 1); SGB(0x200, 1); }
        SGB(0x200, 2);
        SB();
        wait_lgkm0();
    }
    STAMP(2);
    {
        constexpr int sl = b_region(11);
        bar();
        SB();
        f32x4 s[4];
        u32x4 kf[2][4], qf[2];
        auto ld_kq = [&](int k6) {
            const int o = (k6 >> 1) * 8192;
            qf[k6 & 1] = lds_b128(smem + aQ[k6 & 1] + o);
#pragma unroll
            for (int t = 0; t < 4; ++t) kf[k6 & 1][t] = lds_b128(smem + aK[k6 & 1] + o + t * 2048);
        };
        ld_kq(0);
        ld_kq(1);
        {
            constexpr bool Z = false; const Frags& c = f1;
            MB0; MB1; MB2; MB3; MB4; MB5; MB6; MB7; MB8; MB9;
        }
        SB();
#pragma unroll
        for (int k6 = 0; k6 < 6; ++k6) {
#pragma unroll
            for (int t = 0; t < 4; ++t) s[t] = mfma(kf[k6 & 1][t], qf[k6 & 1], k6 == 0 ? f32x4{0.f, 0.f, 0.f, 0.f} : s[t]);
            SB();
            if (k6 + 2 < 6) ld_kq(k6 + 2);
            else if (k6 == 4) { Frags& n = f0; const int ks = 0; LD_B(, , , , , , , , , ) }
            SB();
        }
        float mx = fmaxf(fmaxf(fmaxf(s[0][0], s[0][1]), fmaxf(s[0][2], s[0][3])), fmaxf(fmaxf(s[1][0], s[1][1]), fmaxf(s[1][2], s[1][3])));
        mx = fmaxf(mx, fmaxf(fmaxf(fmaxf(s[2][0], s[2][1]), fmaxf(s[2][2], s[2][3])), fmaxf(fmaxf(s[3][0], s[3][1]), fmaxf(s[3][2], s[3][3]))));
        mx = xmax(mx);
        constexpr float C = 0.07216878364870322f * 1.4426950408889634f;
        const float mc = mx * C;
#pragma unroll
        for (int t = 0; t < 4; ++t)
#pragma unroll
            for (int i = 0; i < 4; ++i) s[t][i] = __builtin_amdgcn_exp2f(fmaf(s[t][i], C, -mc));
        const f32x4 s01 = s[0] + s[1], s23 = s[2] + s[3], s4 = s01 + s23;
        const float rs = __builtin_amdgcn_rcpf(xsum((s4[0] + s4[1]) + (s4[2] + s4[3])));
        const u32x4 pf0 = not_empty(pack8(s[0] * rs, s[1] * rs)), pf1 = not_empty(pack8(s[2] * rs, s[3] * rs));
        {
            constexpr bool Z = false; Frags& n = f1; const Frags& c = f0; const int ks = 1;
            LD_B(MB0, MB1, MB2, MB3, MB4, MB5, MB6, MB7, MB8, MB9)
        }
        __builtin_amdgcn_raw_buffer_store_b128(pf0, rws, WS_X + bid * XCH + w * 2048 + lane * 16, 0, AUX_SC1);
        __builtin_amdgcn_raw_buffer_store_b128(pf1, rws, WS_X + bid * XCH + w * 2048 + 1024 + lane * 16, 0, AUX_SC1);
        *(LAS u32x4*)(smem + aPW) = pf0;
        *(LAS u32x4*)(smem + aPW + 1024) = pf1;
#pragma unroll
        for (int i = 0; i < 10; ++i) { SGB(0x8, 1); SGB(0x100, 1); SGB(0x2, 9); }
        SB();
        wait_lgkm0();
    }
    auto stage = [&](auto S_) {
        constexpr int s = decltype(S_)::value;
        constexpr int c3 = s % 3;
        constexpr int sl = b_region(10 + s);
        bar();
        grp_BB(FF{}, f0, f1, sl, 0);
        if constexpr (c3 == 0) cvt_vw(vw[s / 3 - 1]);
        u32x4 pf[4], pg[4];
        {
            constexpr bool Z = c3 == 0; Frags& n = f1; const Frags& c = f0; const int ks = 1;
            SB();
            if constexpr (s == 8 || s == 9) { load_P(pf, s - 8, 0); load_P(pg, s - 8, 1); }
            if constexpr (s == 10 || s == 11) load_P(pf, 2, s - 10);
            LD_B(MB0, MB1, MB2, MB3, MB4, MB5, MB6, MB7, MB8, MB9)
            if constexpr (s == 8 || s == 9) { mma_C(vw[s - 8], pf, 0); mma_C(vw[s - 8], pg, 1); }
            if constexpr (s == 10 || s == 11) mma_C(vw[2], pf, s - 10);
            if constexpr (s == 8 || s == 9) {
#pragma unroll
                for (int i = 0; i < 9; ++i) { SGB(0x8, 1); SGB(0x100, 2); }
                SGB(0x8, 21);
            } else if constexpr (s >= 10) {
#pragma unroll
                for (int i = 0; i < 6; ++i) { SGB(0x8, 1); SGB(0x100, 2); }
                SGB(0x8, 1); SGB(0x100, 1); SGB(0x8, 1); SGB(0x100, 1);
                SGB(0x8, 14);
            } else ILV();
            SB();
        }
        wait_lgkm0();
    };
    static_for<2, 12>(stage);
    STAMP(3);
    mma_B(f1);
    {
        u32x4 pa[4], pb[4];
        load_P(pa, 3, 0);
        load_P(pb, 3, 1);
        cvt_vw(vw[3]);
        mma_C(vw[3], pa, 0);
        mma_C(vw[3], pb, 1);
    }
    STAMP(4);

    {
        const unsigned ob = WS_X + ((bid & ~7) | (l15 >> 2)) * XCH + 8192 + g * INB + (lg * 4 + (l15 & 3)) * 16;
#pragma unroll
        for (int u = 0; u < 3; ++u)
#pragma unroll
            for (int m = 0; m < 2; ++m)
                __builtin_amdgcn_raw_buffer_store_b128(pack8(oacc[u][2 * m], oacc[u][2 * m + 1]), rws,
                                                       ob + m * (4 * XCH) + (u < 2 ? 2 * w + u : 8 + w) * 256, 0, AUX_SC1);
        STAMP(5);
        wait_vm<0>();
        STAMP(6);
        if (lane == 0) __hip_atomic_store((gu32*)(ws + WS_OF + bid * 128 + w * 4), epoch, RLX_AGENT);
        if (w == 0) {
            const unsigned fo = WS_OF + ((bid & ~7) | (lane & 7)) * 128;
#pragma unroll 1
            for (unsigned spin = 0; spin < SPIN_CAP; ++spin) {
                asm volatile("" ::: "memory");
                const u32x4 f = __builtin_amdgcn_raw_buffer_load_b128(rws, fo, 0, AUX_SC1);
                if (__all(((f[0] ^ epoch) | (f[1] ^ epoch) | (f[2] ^ epoch) | (f[3] ^ epoch)) == 0)) break;
                __builtin_amdgcn_s_sleep(1);
            }
            __builtin_amdgcn_fence(__ATOMIC_ACQUIRE, "wavefront");
        }
        STAMP(7);
        bar();
        if (w < 3) {
            const int T = w * 64 + lane;
            u32x4 v[8];
#pragma unroll
            for (int s = 0; s < 8; ++s)
                v[s] = __builtin_amdgcn_raw_buffer_load_b128(rws, WS_X + bid * XCH + 8192 + s * INB + T * 16, 0, AUX_SC1);
            float r[8];
#pragma unroll
            for (int i = 0; i < 4; ++i) { r[2 * i] = bflo(v[0][i]); r[2 * i + 1] = bfhi(v[0][i]); }
#pragma unroll
            for (int s = 1; s < 8; ++s)
#pragma unroll
                for (int i = 0; i < 4; ++i) { r[2 * i] += bflo(v[s][i]); r[2 * i + 1] += bfhi(v[s][i]); }
            if (w == 2) {
#pragma unroll
                for (int i = 0; i < 8; ++i) r[i] = xsum32(r[i]);
            }
            if (T < 160) {
                const int U = T >> 4, gg = (T >> 2) & 3, qq = T & 3;
                const int row = 32 * (g >> 2) + 4 * (g & 3) + qq;
                const int col = (j & 1) ? (U < 8 ? 32 + 16 * U : 16 * (U - 8)) : 16 * U;
                u16* o = out + (b * SQ + row) * DM + j * 160 + col + 4 * gg;
                *(u32x2*)o = u32x2{pk2(r[0], r[1]), pk2(r[2], r[3])};
                *(u32x2*)(o + 16 * DM) = u32x2{pk2(r[4], r[5]), pk2(r[6], r[7])};
            }
        }
        STAMP(8);
        STAMP(9);
        STAMP_OUT();
        if (g == 0 && j == 0 && threadIdx.x == 0) __hip_atomic_store((gu32*)(ws + WS_E + b * 128), epoch, RLX_AGENT);
    }
}

extern "C" void kernel_launch(void* const* d_in, const int* in_sizes, int n_in, void* d_out, int out_size,
                              void* d_ws, size_t ws_size, hipStream_t stream) {
    mha_fused<<<256, 512, 0, stream>>>((const u16*)d_in[0], (const u16*)d_in[1], (const u16*)d_in[2],
                                       (const u16*)d_in[3], (const u16*)d_in[4], (u16*)d_out, (char*)d_ws);
}
```

```cpp
#include <hip/hip_runtime.h>
#include <stdint.h>
#include <type_traits>

#define LAS __attribute__((address_space(3)))
#define GAS __attribute__((address_space(1)))
typedef unsigned short u16;
typedef short s16x4 __attribute__((ext_vector_type(4)));
typedef short s16x8 __attribute__((ext_vector_type(8)));
typedef float f32x4 __attribute__((ext_vector_type(4)));
typedef float f32x2 __attribute__((ext_vector_type(2)));
typedef __bf16 bf16x2 __attribute__((ext_vector_type(2)));
typedef unsigned u32x4 __attribute__((ext_vector_type(4)));
typedef unsigned u32x2 __attribute__((ext_vector_type(2)));
typedef GAS unsigned gu32;
#define RLX_AGENT __ATOMIC_RELAXED, __HIP_MEMORY_SCOPE_AGENT
#define DI __device__ __forceinline__

namespace {

constexpr int NB = 8, SQ = 64, DM = 640, NH = 32, SK = 64, DH = 192, HD = NH * DH;
constexpr int AUX_SC1 = 16;
constexpr int AUX_NT = 2;

constexpr int SLOT = 32768;
constexpr int L_K = 3 * SLOT;
constexpr int L_I10 = L_K + 24576;
constexpr int L_Q = 1 * SLOT;
constexpr int L_POWN = L_I10 + 28672;
constexpr int LDS_BYTES = 163840;

constexpr unsigned WS_E = 0;
constexpr unsigned WS_OF = 4096 + 256 * 128;
constexpr unsigned WS_RDY = 4096;
constexpr unsigned WS_X = 131072;
constexpr unsigned INB = 3072;
constexpr unsigned XCH = 8192 + 8 * INB;
constexpr unsigned WS_END = WS_X + 256 * XCH;
constexpr unsigned SPIN_CAP = 1u << 16;

template <int N> DI void wait_vm() { asm volatile("s_waitcnt vmcnt(%0)" ::"i"(N) : "memory"); }
DI void bar() { asm volatile("s_barrier" ::: "memory"); }
#define SB() __builtin_amdgcn_sched_barrier(0)
#define SGB(mask, n) __builtin_amdgcn_sched_group_barrier(mask, n, 0)
DI void wait_lgkm0() { __builtin_amdgcn_s_waitcnt(0xC07F); asm volatile("" ::: "memory"); }

DI f32x4 mfma(u32x4 a, u32x4 b, f32x4 c) {
    return __builtin_amdgcn_mfma_f32_16x16x32_bf16(__builtin_bit_cast(s16x8, a), __builtin_bit_cast(s16x8, b), c, 0, 0, 0);
}
DI void mf(f32x4& c, u32x4 a, u32x4 b) { c = mfma(a, b, c); }
template <bool Z> DI void mfz(f32x4& c, u32x4 a, u32x4 b) { c = mfma(a, b, Z ? f32x4{0.f, 0.f, 0.f, 0.f} : c); }
DI int opq(int v) { asm volatile("" : "+v"(v)); return v; }
DI u32x2 trd(LAS char* p) { return __builtin_bit_cast(u32x2, __builtin_amdgcn_ds_read_tr16_b64_v4i16((LAS s16x4*)p)); }
DI void set_lo(u32x4& v, u32x2 x) { v[0] = x[0]; v[1] = x[1]; }
DI void set_hi(u32x4& v, u32x2 x) { v[2] = x[0]; v[3] = x[1]; }
DI u32x4 lds_b128(LAS char* p) { return *(LAS u32x4*)p; }
DI unsigned pk2(float a, float b) {
    f32x2 v = {a, b};
    return __builtin_bit_cast(unsigned, __builtin_convertvector(v, bf16x2));
}
DI u32x4 pack8(f32x4 a, f32x4 b) { return u32x4{pk2(a[0], a[1]), pk2(a[2], a[3]), pk2(b[0], b[1]), pk2(b[2], b[3])}; }
DI float bflo(unsigned w) { return __uint_as_float(w << 16); }
DI float bfhi(unsigned w) { return __uint_as_float(w & 0xffff0000u); }
DI float xmax(float v) {
    auto a = __builtin_amdgcn_permlane16_swap(__float_as_uint(v), __float_as_uint(v), false, false);
    v = fmaxf(__uint_as_float(a[0]), __uint_as_float(a[1]));
    auto c = __builtin_amdgcn_permlane32_swap(__float_as_uint(v), __float_as_uint(v), false, false);
    return fmaxf(__uint_as_float(c[0]), __uint_as_float(c[1]));
}
DI float xsum(float v) {
    auto a = __builtin_amdgcn_permlane16_swap(__float_as_uint(v), __float_as_uint(v), false, false);
    v = __uint_as_float(a[0]) + __uint_as_float(a[1]);
    auto c = __builtin_amdgcn_permlane32_swap(__float_as_uint(v), __float_as_uint(v), false, false);
    return __uint_as_float(c[0]) + __uint_as_float(c[1]);
}
DI float xsum32(float v) {
    auto c = __builtin_amdgcn_permlane32_swap(__float_as_uint(v), __float_as_uint(v), false, false);
    return __uint_as_float(c[0]) + __uint_as_float(c[1]);
}

struct Frags { u32x4 nat[4]; u32x4 tr[3]; };
template <int I, int N, class F> DI void static_for(F&& f) {
    if constexpr (I < N) { f(std::integral_constant<int, I>{}); static_for<I + 1, N>(f); }
}

DI unsigned sload(const char* p) { unsigned v; asm volatile("s_load_dword %0, %1, 0x0\n\ts_waitcnt lgkmcnt(0)" : "=s"(v) : "s"(p) : "memory"); return v; }
DI void gld_sc1(u32x4& d, __amdgpu_buffer_rsrc_t r, unsigned voff) {
    asm volatile("buffer_load_dwordx4 %0, %1, %2, 0 offen sc1" : "=v"(d) : "v"(voff), "s"(r) : "memory");
}
template <int AUX> DI void dma1(__amdgpu_buffer_rsrc_t r, unsigned lds_dst, unsigned voff, unsigned soff) {
    if (AUX == 2) asm volatile("s_mov_b32 m0, %0\n\ts_nop 0\n\tbuffer_load_dwordx4 %1, %2, %3 offen nt lds" :: "s"(lds_dst), "v"(voff), "s"(r), "s"(soff) : "memory");
    else asm volatile("s_mov_b32 m0, %0\n\ts_nop 0\n\tbuffer_load_dwordx4 %1, %2, %3 offen lds" :: "s"(lds_dst), "v"(voff), "s"(r), "s"(soff) : "memory");
}
DI void landed(u32x4& a) { asm volatile("" : "+v"(a) :: "memory"); }

constexpr int PF_LO = 2, PF_SPAN = 6, PF_B1 = 40;
static_assert(PF_LO + PF_SPAN - 1 <= 8 && PF_B1 >= 28 && PF_B1 <= 52, "");
constexpr int b_region(int it) { return it == 10 ? 4 : (it == 11 ? 2 : (it == 12 ? 0 : ((it - 13) % 4 == 0 ? 4 : ((it - 13) % 4 == 1 ? 1 : ((it - 13) % 4 == 2 ? 2 : 0))))); }
constexpr int region_base(int r) { return r == 4 ? L_I10 : r * SLOT; }

constexpr unsigned EMPTY = 0xffffffffu;
DI u32x4 not_empty(u32x4 v) { return u32x4{min(v[0], EMPTY - 1), min(v[1], EMPTY - 1), min(v[2], EMPTY - 1), min(v[3], EMPTY - 1)}; }
DI unsigned umax4(u32x4 v) { return max(max(v[0], v[1]), max(v[2], v[3])); }

}

__global__ __launch_bounds__(512, 2) void mha_fused(const u16* __restrict__ x, const u16* __restrict__ kc,
                                                    const u16* __restrict__ vc, const u16* __restrict__ wq,
                                                    const u16* __restrict__ wo, u16* __restrict__ out,
                                                    char* __restrict__ ws) {
    __shared__ __attribute__((aligned(16))) char smem_[LDS_BYTES];
    LAS char* smem = (LAS char*)smem_;
    const int bid = blockIdx.x;
    const int g = bid & 7, j = (bid >> 3) & 3, b = bid >> 5;
    const int h = 4 * g + j;
    const int wave = __builtin_amdgcn_readfirstlane(threadIdx.x >> 6);
    const int lane = threadIdx.x & 63;
    int ksA0 = ((b * 10) >> 3) + ((j * 5) >> 1); if (ksA0 >= 10) ksA0 -= 10;
    const int hrot = (j + b) & 3;
    const int crot = b >> 2;
    const int r8 = lane >> 3, c8 = lane & 7;
    const unsigned sw8 = (unsigned)((c8 ^ r8) * 16);
    const unsigned swt = (unsigned)((2 * ((c8 >> 1) ^ ((r8 >> 1) & 1)) + (c8 & 1)) * 16);
    const unsigned vx = (unsigned)(r8 * (DM * 2)) + sw8;
    const __amdgpu_buffer_rsrc_t rx = __builtin_amdgcn_make_buffer_rsrc((void*)x, 0, NB * SQ * DM * 2, 0x00020000);
    const __amdgpu_buffer_rsrc_t rq = __builtin_amdgcn_make_buffer_rsrc((void*)wq, 0, DM * HD * 2, 0x00020000);
#define DMA(rs, dst, voff, soff) __builtin_amdgcn_raw_ptr_buffer_load_lds(rs, (LAS void*)(dst), 16, voff, soff, 0, 0)

    {
        const unsigned vq = (unsigned)(r8 * (HD * 2)) + (swt ^ (unsigned)((wave & 1) * 64));
        const unsigned sx = (unsigned)((b * 64 + 8 * wave) * (DM * 2)), so = (unsigned)(8 * wave * (HD * 2) + h * (DH * 2));
#pragma unroll
        for (int it = 0; it < 2; ++it) {
            LAS char* sb = smem + it * SLOT + wave * 1024;
            const int ks = ksA0 + it == 10 ? 0 : ksA0 + it;
#pragma unroll
            for (int blk = 0; blk < 3; ++blk) DMA(rq, sb + 8192 + blk * 8192, vq, so + ks * 64 * (HD * 2) + blk * 128);
            DMA(rx, sb, vx, sx + ks * 128);
        }
    }

    if (wave >= 4) {
        const __amdgpu_buffer_rsrc_t rv = __builtin_amdgcn_make_buffer_rsrc((void*)vc, 0, NB * NH * SK * DH * 2, 0x00020000);
        const __amdgpu_buffer_rsrc_t ro = __builtin_amdgcn_make_buffer_rsrc((void*)wo, 0, HD * DM * 2, 0x00020000);
        const unsigned vkv = (unsigned)(r8 * (DH * 2)) + sw8;
        const unsigned vh = (unsigned)((lane >> 2) * (DM * 2) + ((lane & 3) ^ ((lane >> 4) & 2)) * 16);
        const unsigned colF = (unsigned)(j * 320 + (j & 1) * 64), colH = (unsigned)(j * 320 + ((j & 1) ? 0 : 256));
        auto stage_hh = [&](int s) { return 4 * g + ((hrot + s / 3) & 3); };
        auto stage_ch = [&](int s) { int c = s % 3 + crot; return c >= 3 ? c - 3 : c; };
        const __amdgpu_buffer_rsrc_t rws = __builtin_amdgcn_make_buffer_rsrc((void*)ws, 0, WS_END, 0x00020000);
        if (wave < 7) {
            const int lw = wave - 4;
            const unsigned swA = swt ^ (unsigned)((lw & 1) * 64), swB = swA ^ 64u;
            const unsigned vqA = (unsigned)(r8 * (HD * 2)) + swA, vqB = (unsigned)(r8 * (HD * 2)) + swB;
            const unsigned voA = (unsigned)(r8 * (DM * 2)) + swA, voB = (unsigned)(r8 * (DM * 2)) + swB;
            LAS char* const sw = smem + lw * 1024;
            const unsigned sx0 = (unsigned)((b * 64 + 8 * lw) * (DM * 2));
            const unsigned sq0 = (unsigned)(8 * lw * (HD * 2) + h * (DH * 2));
            auto run = [&](auto THREE) {
                constexpr bool three = decltype(THREE)::value;
                constexpr int NA = three ? 12 : 8, NBI = three ? 9 : 10, NP = three ? 3 : 2;
                wait_vm<4>();
                static_for<2, 10>([&](auto IT) {
                    constexpr int it = decltype(IT)::value;
                    int ks = ksA0 + it; if (ks >= 10) ks -= 10;
                    const unsigned sx = sx0 + ks * 128, so = sq0 + ks * 64 * (HD * 2);
                    LAS char* sb = sw + (it % 3) * SLOT;
                    SB();
                    if constexpr (it > 2) wait_vm<NA>();
                    bar();
                    SB();
#pragma unroll
                    for (int rg = 0; rg < (three ? 3 : 2); ++rg) {
                        DMA(rx, sb + rg * 3072, vx, sx + rg * 24 * DM * 2);
#pragma unroll
                        for (int blk = 0; blk < 3; ++blk)
                            DMA(rq, sb + 8192 + blk * 8192 + rg * 3072, rg == 1 ? vqB : vqA, so + blk * 128 + rg * 24 * HD * 2);
                    }
                });
                wait_vm<NA>(); bar();
                static_for<11, 22>([&](auto IT) {
                    constexpr int it = decltype(IT)::value;
                    const int hh = stage_hh(it - 10), ch = stage_ch(it - 10);
                    const unsigned sv = (unsigned)(((b * NH + hh) * SK + 8 * lw) * (DH * 2) + ch * 128);
                    const unsigned so = (unsigned)((hh * DH + ch * 64) * (DM * 2));
                    LAS char* sb = sw + region_base(b_region(it));
                    SB();
                    if constexpr (it == 11) wait_vm<0>();
                    else if constexpr (it > 12) {
                        if constexpr (it - 1 >= 17 && it - 1 < 21) { if (((b + it - 18) & 3) != 0) wait_vm<NBI + NP>(); else wait_vm<NBI>(); }
                        else wait_vm<NBI>();
                    }
                    bar();
                    SB();
#pragma unroll
                    for (int rg = 0; rg < (three ? 3 : 2); ++rg) {
                        DMA(rv, sb + rg * 3072, vkv, sv + rg * 24 * DH * 2);
#pragma unroll
                        for (int blk = 0; blk < 2; ++blk)
                            DMA(ro, sb + 8192 + blk * 8192 + rg * 3072, rg == 1 ? voB : voA, so + colF + blk * 128 + (8 * lw + rg * 24) * DM * 2);
                    }
                    if constexpr (!three) {
#pragma unroll
                        for (int q = 0; q < 4; ++q) DMA(ro, sb - lw * 1024 + 24576 + q * 1024, vh, so + colH + q * 16 * DM * 2);
                    }
                    if constexpr (it >= 17 && it < 21) {
                        const int d = (b + it - 17) & 3;
                        if (d != 0) {
                            const unsigned src = WS_X + (unsigned)(((bid & ~24) | (((j + d) & 3) << 3)) * XCH);
#pragma unroll
                            for (int i = 0; i < NP; ++i)
                                __builtin_amdgcn_raw_ptr_buffer_load_lds(rws, (LAS void*)(smem + L_K + (d - 1) * 8192 + (lw + 3 * i) * 1024), 16,
                                                                         (unsigned)(lane * 16), src + (lw + 3 * i) * 1024, 0, AUX_SC1);
                        }
                    }
                });
                wait_vm<NBI>(); bar();
                wait_vm<0>(); bar();
            };
            if (lw < 2) run(std::true_type{}); else run(std::false_type{});
        } else {
            const __amdgpu_buffer_rsrc_t rk = __builtin_amdgcn_make_buffer_rsrc((void*)kc, 0, NB * NH * SK * DH * 2, 0x00020000);
            const unsigned vo0 = (unsigned)(r8 * (DM * 2)) + swt, vo1 = (unsigned)(r8 * (DM * 2)) + (swt ^ 64u);
            auto piece_load = [&](auto N_) {
                constexpr int n = decltype(N_)::value;
                if constexpr (n < 28) {
                    const int hh = stage_hh(0), ch = stage_ch(0);
                    const unsigned so = (unsigned)((hh * DH + ch * 64) * (DM * 2));
                    constexpr unsigned dst = L_I10 + n * 1024;
                    if constexpr (n < 8) dma1<0>(rv, dst, vkv, (unsigned)(((b * NH + hh) * SK + n * 8) * (DH * 2) + ch * 128));
                    else if constexpr (n < 24) dma1<0>(ro, dst, (n & 1) ? vo1 : vo0, so + colF + ((n - 8) >> 3) * 128 + (n & 7) * 8 * (DM * 2));
                    else dma1<0>(ro, dst, vh, so + colH + (n - 24) * 16 * (DM * 2));
                } else {
                    constexpr int st = (n - 28) / 8, rg = (n - 28) % 8;
                    dma1<AUX_NT>(rk, (unsigned)(L_K + st * 8192 + rg * 1024), vkv, (unsigned)(((b * NH + h) * SK + rg * 8) * (DH * 2) + st * 128));
                }
            };
            const unsigned epoch = sload(ws + WS_E + b * 128) + 1u;
            wait_vm<4>();
            const int tb = PF_LO + (bid >> 3) % PF_SPAN;
            const u32x4 e4 = {EMPTY, EMPTY, EMPTY, EMPTY};
            bar();
            wait_vm<0>();
            bar();
#pragma unroll 1
            for (int t = 2; t < 9; ++t) {
                bar();
                if (t == tb) {
                    static_for<0, PF_B1>(piece_load);
#pragma unroll
                    for (int i = 0; i < 8; ++i)
                        __builtin_amdgcn_raw_buffer_store_b128(e4, rws, WS_X + bid * XCH + i * 1024 + lane * 16, 0, AUX_SC1);
                }
            }
            bar();
            static_for<PF_B1, 52>(piece_load);
            wait_vm<52 - PF_B1>();
            if (lane == 0) __hip_atomic_store((gu32*)(ws + WS_RDY + bid * 128), epoch, RLX_AGENT);
            const int peer = (bid & ~24) | (((j + 1 + (lane & 3)) & 3) << 3);
            unsigned rdy;
            const int aPH = opq(L_K + lane * 16);
            static_for<10, 22>([&](auto T_) {
                constexpr int t = decltype(T_)::value;
                bar();
                if constexpr (t == 10) wait_vm<0>();
                if constexpr (t == 13) {
                    asm volatile("buffer_load_dword %0, %1, %2, 0 offen sc1" : "=v"(rdy) : "v"(WS_RDY + peer * 128), "s"(rws) : "memory");
                }
                if constexpr (t == 14) {
                    asm volatile("s_waitcnt vmcnt(0)" : "+v"(rdy) :: "memory");
#pragma unroll 1
                    for (unsigned spin = 0; spin < SPIN_CAP && !__all(rdy == epoch); ++spin) {
                        __builtin_amdgcn_s_sleep(1);
                        asm volatile("buffer_load_dword %0, %1, %2, 0 offen sc1\n\ts_waitcnt vmcnt(0)" : "=&v"(rdy) : "v"(WS_RDY + peer * 128), "s"(rws) : "memory");
                    }
                }
                if constexpr (t >= 17 && t < 21) {
                    constexpr int m = t - 17;
                    const int d = (b + m) & 3;
                    if (d != 0) {
                        unsigned mx = 0;
#pragma unroll
                        for (int i = 0; i < 8; ++i) mx = max(mx, umax4(*(LAS u32x4*)(smem + aPH + (d - 1) * 8192 + i * 1024)));
                        if (!__all(mx != EMPTY)) {
                            u32x4 p[8];
#pragma unroll 1
                            for (unsigned spin = 0; spin < SPIN_CAP; ++spin) {
#pragma unroll
                                for (int i = 0; i < 8; ++i)
                                    gld_sc1(p[i], rws, WS_X + ((bid & ~24) | (((j + d) & 3) << 3)) * XCH + i * 1024 + lane * 16);
                                wait_vm<0>();
                                mx = 0;
#pragma unroll
                                for (int i = 0; i < 8; ++i) { landed(p[i]); mx = max(mx, umax4(p[i])); }
                                if (__all(mx != EMPTY)) break;
                            }
#pragma unroll
                            for (int i = 0; i < 8; ++i) *(LAS u32x4*)(smem + aPH + (d - 1) * 8192 + i * 1024) = p[i];
                        }
                    }
                }
                wait_lgkm0();
            });
        }
        return;
    }

    const int w = wave;
    const int l15 = lane & 15, lg = lane >> 4;
    const __amdgpu_buffer_rsrc_t rws = __builtin_amdgcn_make_buffer_rsrc((void*)ws, 0, WS_END, 0x00020000);
    const unsigned epoch = sload(ws + WS_E + b * 128) + 1u;

    const int an0 = l15 * 128 + ((lg ^ (l15 & 7)) << 4);
    const int q4 = l15 >> 2, p4 = l15 & 3;
    const int sq = ((q4 >> 1) & 1) | ((lg & 1) << 1);
    const int kx = (w >> 1) * 4096;
    int aq[2][3], ao[3][3], an[2][2], anA[3][2], anB[3][2];
#pragma unroll
    for (int hi = 0; hi < 3; ++hi) {
        const int base = hi == 2 ? L_I10 : hi * 65536;
        if (hi < 2) {
#pragma unroll
            for (int u = 0; u < 3; ++u) {
                const int T = 3 * w + u;
                aq[hi][u] = opq(base + 8192 + (T >> 2) * 8192 + (8 * lg + q4) * 128 + (((T & 3) ^ sq) << 5) + p4 * 8);
            }
            an[hi][0] = opq(base + an0);
            an[hi][1] = opq(base + (an0 ^ 64));
        }
#pragma unroll
        for (int u = 0; u < 2; ++u) {
            const int T = 2 * w + u;
            ao[hi][u] = opq(base + 8192 + (T >> 2) * 8192 + (8 * lg + q4) * 128 + (((T & 3) ^ sq) << 5) + p4 * 8);
        }
        ao[hi][2] = opq(base + 24576 + (8 * lg + q4) * 64 + (((w & 1) ^ (lg & 1)) << 5) + p4 * 8);
#pragma unroll
        for (int ks = 0; ks < 2; ++ks) {
            anA[hi][ks] = opq(base + (an0 ^ (ks << 6)) + kx);
            anB[hi][ks] = opq(base + (an0 ^ (ks << 6)) + 4096 - kx);
        }
    }

    int aW[3];
#pragma unroll
    for (int u = 0; u < 3; ++u) {
        const int T = 3 * w + u;
        aW[u] = opq(L_Q + (T >> 2) * 8192 + l15 * 128 + (((2 * (T & 3) + (lg >> 1)) ^ (l15 & 7)) << 4) + (lg & 1) * 8);
    }
    const int aPW = opq(L_POWN + w * 2048 + lane * 16);
    const int aK[2] = {opq(L_K + an0), opq(L_K + (an0 ^ 64))};
    const int aQ[2] = {opq(L_Q + w * 2048 + an0), opq(L_Q + w * 2048 + (an0 ^ 64))};
    const int aP[2] = {opq(L_K + lane * 16 + (w >> 1) * 1024), opq(L_K + lane * 16 + 1024 - (w >> 1) * 1024)};
    f32x4 acc[3][4];
#pragma unroll
    for (int u = 0; u < 3; ++u)
#pragma unroll
        for (int t = 0; t < 4; ++t) { acc[u][t] = f32x4{0.f, 0.f, 0.f, 0.f}; asm volatile("" : "+v"(acc[u][t])); }
#define ILV() do { SGB(0x8, 1); SGB(0x100, 2); SGB(0x8, 1); SGB(0x100, 1); SGB(0x8, 1); SGB(0x100, 1); SGB(0x8, 1); SGB(0x100, 1); SGB(0x8, 1); SGB(0x100, 1); \
                   SGB(0x8, 1); SGB(0x100, 2); SGB(0x8, 1); SGB(0x100, 2); SGB(0x8, 3); } while (0)
#define MA(i) mf(acc[(i) / 4][(i) % 4], c.tr[(i) / 4], c.nat[(i) % 4])
#define LPU(reg, u, off) (smem + (reg)[sl >> 1][u] + (sl == 1 ? SLOT : 0) + (off))
#define LD_A(X0, X1, X2, X3, X4, X5, X6, X7, X8, X9)                                                        \
        X0; set_lo(n.tr[0], trd(LPU(aq, 0, ks * 4096))); set_hi(n.tr[0], trd(LPU(aq, 0, ks * 4096 + 512))); \
        X1; n.nat[0] = lds_b128(LPU(an, ks, 0));                                                            \
        X2; n.nat[1] = lds_b128(LPU(an, ks, 2048));                                                         \
        X3; n.nat[2] = lds_b128(LPU(an, ks, 4096));                                                         \
        X4; n.nat[3] = lds_b128(LPU(an, ks, 6144));                                                         \
        X5; set_lo(n.tr[1], trd(LPU(aq, 1, ks * 4096))); set_hi(n.tr[1], trd(LPU(aq, 1, ks * 4096 + 512))); \
        X6; set_lo(n.tr[2], trd(LPU(aq, 2, ks * 4096))); set_hi(n.tr[2], trd(LPU(aq, 2, ks * 4096 + 512))); \
        X7; X8; X9;
#define LD_B(X0, X1, X2, X3, X4, X5, X6, X7, X8, X9)                                                        \
        X0; set_lo(n.tr[0], trd(LPU(ao, 0, ks * 4096))); set_hi(n.tr[0], trd(LPU(ao, 0, ks * 4096 + 512))); \
        X1; n.nat[0] = lds_b128(LPU(anA, ks, 0));                                                           \
            set_lo(n.tr[2], trd(LPU(ao, 2, ks * 2048)));                                                    \
        X2; n.nat[1] = lds_b128(LPU(anA, ks, 2048));                                                        \
            set_hi(n.tr[2], trd(LPU(ao, 2, ks * 2048 + 256)));                                              \
        X3; n.nat[2] = lds_b128(LPU(anB, ks, 0));                                                           \
        X4; n.nat[3] = lds_b128(LPU(anB, ks, 2048));                                                        \
        X5; set_lo(n.tr[1], trd(LPU(ao, 1, ks * 4096))); set_hi(n.tr[1], trd(LPU(ao, 1, ks * 4096 + 512))); \
        X6; X7; X8; X9;
    auto grp_AA = [&](Frags& n, const Frags& c, const int sl, const int ks) {
        SB();
        LD_A(MA(0), MA(1), MA(2), MA(3), MA(4), MA(5), MA(6), MA(7), MA(8), MA(9))
        MA(10); MA(11);
        ILV(); SGB(0x8, 2);
        SB();
    };
    auto grp_BA = [&](Frags& n, const Frags& c, const int sl, const int ks) {
        SB();
        LD_B(MA(0), MA(1), MA(2), MA(3), MA(4), MA(5), MA(6), MA(7), MA(8), MA(9))
        MA(10); MA(11);
        ILV(); SGB(0x8, 2);
        SB();
    };
#define MB0 mfz<Z>(acc[0][0], c.nat[0], c.tr[0])
#define MB1 mfz<Z>(acc[0][1], c.nat[1], c.tr[0])
#define MB2 mfz<Z>(acc[2][0], c.nat[0], c.tr[2])
#define MB3 mfz<Z>(acc[2][1], c.nat[1], c.tr[2])
#define MB4 mfz<Z>(acc[0][2], c.nat[2], c.tr[0])
#define MB5 mfz<Z>(acc[0][3], c.nat[3], c.tr[0])
#define MB6 mfz<Z>(acc[1][0], c.nat[0], c.tr[1])
#define MB7 mfz<Z>(acc[1][1], c.nat[1], c.tr[1])
#define MB8 mfz<Z>(acc[1][2], c.nat[2], c.tr[1])
#define MB9 mfz<Z>(acc[1][3], c.nat[3], c.tr[1])
    auto grp_BB = [&](auto ZZ, Frags& n, const Frags& c, const int sl, const int ks) {
        constexpr bool Z = decltype(ZZ)::value;
        SB();
        LD_B(MB0, MB1, MB2, MB3, MB4, MB5, MB6, MB7, MB8, MB9)
        ILV();
        SB();
    };
    auto mma_B = [&](const Frags& c) { constexpr bool Z = false; MB0; MB1; MB2; MB3; MB4; MB5; MB6; MB7; MB8; MB9; };
    using TT = std::true_type; using FF = std::false_type;

    f32x4 oacc[3][4];
#pragma unroll
    for (int u = 0; u < 3; ++u)
#pragma unroll
        for (int t = 0; t < 4; ++t) { oacc[u][t] = f32x4{0.f, 0.f, 0.f, 0.f}; asm volatile("" : "+v"(oacc[u][t])); }
    Frags f0, f1;
#pragma unroll
    for (int t = 0; t < 4; ++t) f1.nat[t] = u32x4{0, 0, 0, 0};
#pragma unroll
    for (int u = 0; u < 3; ++u) f1.tr[u] = u32x4{0, 0, 0, 0};
    static_for<0, 10>([&](auto T_) {
        constexpr int t = decltype(T_)::value;
        if constexpr (t == 0) wait_vm<4>();
        if constexpr (t == 1) wait_vm<0>();
        bar();
        grp_AA(f0, f1, t % 3, 0);
        grp_AA(f1, f0, t % 3, 1);
        wait_lgkm0();
    });
    u32x4 vw[4][5];
    auto cvt_vw = [&](u32x4 (&v)[5]) {
#pragma unroll
        for (int u = 0; u < 2; ++u) {
            v[2 * u] = pack8(acc[u][0], acc[u][1]);
            v[2 * u + 1] = pack8(acc[u][2], acc[u][3]);
        }
        v[4] = pack8(acc[2][0], acc[2][1]);
    };
    auto load_P = [&](u32x4 (&pf)[4], int m, int p) {
        const int d = (b + m) & 3;
        LAS char* pb = smem + aP[p] + (d == 0 ? L_POWN - L_K : (d - 1) * 8192);
#pragma unroll
        for (int t = 0; t < 4; ++t) pf[t] = lds_b128(pb + t * 2048);
    };
    auto mma_C = [&](const u32x4 (&v)[5], const u32x4 (&pf)[4], int p) {
#pragma unroll
        for (int t = 0; t < 4; ++t) {
            mf(oacc[0][t], v[p], pf[t]);
            mf(oacc[1][t], v[2 + p], pf[t]);
            if (p == 0) mf(oacc[2][t], v[4], pf[t]);
        }
    };
    {
        constexpr int sl = 4;
        bar();
        grp_BA(f0, f1, sl, 0);
        u32x2 qv[3][4];
#pragma unroll
        for (int u = 0; u < 3; ++u)
#pragma unroll
            for (int t = 0; t < 4; ++t) qv[u][t] = u32x2{pk2(acc[u][t][0], acc[u][t][1]), pk2(acc[u][t][2], acc[u][t][3])};
        {
            constexpr bool Z = true;
            Frags& n = f1; const Frags& c = f0; const int ks = 1;
            LD_B(MB0, MB1, MB2, MB3, MB4, MB5, MB6, MB7, MB8, MB9)
        }
#pragma unroll
        for (int u = 0; u < 3; ++u) {
            LAS char* qb = smem + aW[u];
#pragma unroll
            for (int t = 0; t < 4; ++t) *(LAS u32x2*)(qb + t * 2048) = qv[u][t];
        }
        SGB(0x2, 24);
#pragma unroll
        for (int i = 0; i < 10; ++i) { SGB(0x8, 1); SGB(0x100, 1); SGB(0x200, 1); }
        SGB(0x200, 2);
        SB();
        wait_lgkm0();
    }
    {
        constexpr int sl = b_region(11);
        bar();
        SB();
        f32x4 s[4];
        u32x4 kf[2][4], qf[2];
        auto ld_kq = [&](int k6) {
            const int o = (k6 >> 1) * 8192;
            qf[k6 & 1] = lds_b128(smem + aQ[k6 & 1] + o);
#pragma unroll
            for (int t = 0; t < 4; ++t) kf[k6 & 1][t] = lds_b128(smem + aK[k6 & 1] + o + t * 2048);
        };
        ld_kq(0);
        ld_kq(1);
        {
            constexpr bool Z = false; const Frags& c = f1;
            MB0; MB1; MB2; MB3; MB4; MB5; MB6; MB7; MB8; MB9;
        }
        SB();
#pragma unroll
        for (int k6 = 0; k6 < 6; ++k6) {
#pragma unroll
            for (int t = 0; t < 4; ++t) s[t] = mfma(kf[k6 & 1][t], qf[k6 & 1], k6 == 0 ? f32x4{0.f, 0.f, 0.f, 0.f} : s[t]);
            SB();
            if (k6 + 2 < 6) ld_kq(k6 + 2);
            else if (k6 == 4) { Frags& n = f0; const int ks = 0; LD_B(, , , , , , , , , ) }
            SB();
        }
        float mx = fmaxf(fmaxf(fmaxf(s[0][0], s[0][1]), fmaxf(s[0][2], s[0][3])), fmaxf(fmaxf(s[1][0], s[1][1]), fmaxf(s[1][2], s[1][3])));
        mx = fmaxf(mx, fmaxf(fmaxf(fmaxf(s[2][0], s[2][1]), fmaxf(s[2][2], s[2][3])), fmaxf(fmaxf(s[3][0], s[3][1]), fmaxf(s[3][2], s[3][3]))));
        mx = xmax(mx);
        constexpr float C = 0.07216878364870322f * 1.4426950408889634f;
        const float mc = mx * C;
#pragma unroll
        for (int t = 0; t < 4; ++t)
#pragma unroll
            for (int i = 0; i < 4; ++i) s[t][i] = __builtin_amdgcn_exp2f(fmaf(s[t][i], C, -mc));
        const f32x4 s01 = s[0] + s[1], s23 = s[2] + s[3], s4 = s01 + s23;
        const float rs = __builtin_amdgcn_rcpf(xsum((s4[0] + s4[1]) + (s4[2] + s4[3])));
        const u32x4 pf0 = not_empty(pack8(s[0] * rs, s[1] * rs)), pf1 = not_empty(pack8(s[2] * rs, s[3] * rs));
        {
            constexpr bool Z = false; Frags& n = f1; const Frags& c = f0; const int ks = 1;
            LD_B(MB0, MB1, MB2, MB3, MB4, MB5, MB6, MB7, MB8, MB9)
        }
        __builtin_amdgcn_raw_buffer_store_b128(pf0, rws, WS_X + bid * XCH + w * 2048 + lane * 16, 0, AUX_SC1);
        __builtin_amdgcn_raw_buffer_store_b128(pf1, rws, WS_X + bid * XCH + w * 2048 + 1024 + lane * 16, 0, AUX_SC1);
        *(LAS u32x4*)(smem + aPW) = pf0;
        *(LAS u32x4*)(smem + aPW + 1024) = pf1;
#pragma unroll
        for (int i = 0; i < 10; ++i) { SGB(0x8, 1); SGB(0x100, 1); SGB(0x2, 9); }
        SB();
        wait_lgkm0();
    }
    auto stage = [&](auto S_) {
        constexpr int s = decltype(S_)::value;
        constexpr int c3 = s % 3;
        constexpr int sl = b_region(10 + s);
        bar();
        grp_BB(FF{}, f0, f1, sl, 0);
        if constexpr (c3 == 0) cvt_vw(vw[s / 3 - 1]);
        u32x4 pf[4], pg[4];
        {
            constexpr bool Z = c3 == 0; Frags& n = f1; const Frags& c = f0; const int ks = 1;
            SB();
            if constexpr (s == 8 || s == 9) { load_P(pf, s - 8, 0); load_P(pg, s - 8, 1); }
            if constexpr (s == 10 || s == 11) load_P(pf, 2, s - 10);
            LD_B(MB0, MB1, MB2, MB3, MB4, MB5, MB6, MB7, MB8, MB9)
            if constexpr (s == 8 || s == 9) { mma_C(vw[s - 8], pf, 0); mma_C(vw[s - 8], pg, 1); }
            if constexpr (s == 10 || s == 11) mma_C(vw[2], pf, s - 10);
            if constexpr (s == 8 || s == 9) {
#pragma unroll
                for (int i = 0; i < 9; ++i) { SGB(0x8, 1); SGB(0x100, 2); }
                SGB(0x8, 21);
            } else if constexpr (s >= 10) {
#pragma unroll
                for (int i = 0; i < 6; ++i) { SGB(0x8, 1); SGB(0x100, 2); }
                SGB(0x8, 1); SGB(0x100, 1); SGB(0x8, 1); SGB(0x100, 1);
                SGB(0x8, 14);
            } else ILV();
            SB();
        }
        wait_lgkm0();
    };
    static_for<2, 12>(stage);
    mma_B(f1);
    {
        u32x4 pa[4], pb[4];
        load_P(pa, 3, 0);
        load_P(pb, 3, 1);
        cvt_vw(vw[3]);
        mma_C(vw[3], pa, 0);
        mma_C(vw[3], pb, 1);
    }

    {
        const unsigned ob = WS_X + ((bid & ~7) | (l15 >> 2)) * XCH + 8192 + g * INB + (lg * 4 + (l15 & 3)) * 16;
#pragma unroll
        for (int u = 0; u < 3; ++u)
#pragma unroll
            for (int m = 0; m < 2; ++m)
                __builtin_amdgcn_raw_buffer_store_b128(pack8(oacc[u][2 * m], oacc[u][2 * m + 1]), rws,
                                                       ob + m * (4 * XCH) + (u < 2 ? 2 * w + u : 8 + w) * 256, 0, AUX_SC1);
        wait_vm<0>();
        if (lane == 0) __hip_atomic_store((gu32*)(ws + WS_OF + bid * 128 + w * 4), epoch, RLX_AGENT);
        if (w == 0) {
            const unsigned fo = WS_OF + ((bid & ~7) | (lane & 7)) * 128;
#pragma unroll 1
            for (unsigned spin = 0; spin < SPIN_CAP; ++spin) {
                asm volatile("" ::: "memory");
                const u32x4 f = __builtin_amdgcn_raw_buffer_load_b128(rws, fo, 0, AUX_SC1);
                if (__all(((f[0] ^ epoch) | (f[1] ^ epoch) | (f[2] ^ epoch) | (f[3] ^ epoch)) == 0)) break;
                __builtin_amdgcn_s_sleep(1);
            }
            __builtin_amdgcn_fence(__ATOMIC_ACQUIRE, "wavefront");
        }
        bar();
        if (w < 3) {
            const int T = w * 64 + lane;
            u32x4 v[8];
#pragma unroll
            for (int s = 0; s < 8; ++s)
                v[s] = __builtin_amdgcn_raw_buffer_load_b128(rws, WS_X + bid * XCH + 8192 + s * INB + T * 16, 0, AUX_SC1);
            float r[8];
#pragma unroll
            for (int i = 0; i < 4; ++i) { r[2 * i] = bflo(v[0][i]); r[2 * i + 1] = bfhi(v[0][i]); }
#pragma unroll
            for (int s = 1; s < 8; ++s)
#pragma unroll
                for (int i = 0; i < 4; ++i) { r[2 * i] += bflo(v[s][i]); r[2 * i + 1] += bfhi(v[s][i]); }
            if (w == 2) {
#pragma unroll
                for (int i = 0; i < 8; ++i) r[i] = xsum32(r[i]);
            }
            if (T < 160) {
                const int U = T >> 4, gg = (T >> 2) & 3, qq = T & 3;
                const int row = 32 * (g >> 2) + 4 * (g & 3) + qq;
                const int col = (j & 1) ? (U < 8 ? 32 + 16 * U : 16 * (U - 8)) : 16 * U;
                u16* o = out + (b * SQ + row) * DM + j * 160 + col + 4 * gg;
                *(u32x2*)o = u32x2{pk2(r[0], r[1]), pk2(r[2], r[3])};
                *(u32x2*)(o + 16 * DM) = u32x2{pk2(r[4], r[5]), pk2(r[6], r[7])};
            }
        }
        if (g == 0 && j == 0 && threadIdx.x == 0) __hip_atomic_store((gu32*)(ws + WS_E + b * 128), epoch, RLX_AGENT);
    }
}

extern "C" void kernel_launch(void* const* d_in, const int* in_sizes, int n_in, void* d_out, int out_size,
                              void* d_ws, size_t ws_size, hipStream_t stream) {
    mha_fused<<<256, 512, 0, stream>>>((const u16*)d_in[0], (const u16*)d_in[1], (const u16*)d_in[2],
                                       (const u16*)d_in[3], (const u16*)d_in[4], (u16*)d_out, (char*)d_ws);
}
```
